```python
import torch

def _softmax(x, axis=-1):
    return torch.softmax(x, dim=axis)
def _gelu(x):
    return torch.nn.functional.gelu(x, approximate="tanh")
def _silu(x):
    return torch.nn.functional.silu(x)
def _sigmoid(x):
    return torch.sigmoid(x)
def _layernorm(x, axis=-1, eps=1e-5):
    m = x.mean(dim=axis, keepdim=True)
    v = x.var(dim=axis, keepdim=True, unbiased=False)
    return (x - m) / torch.sqrt(v + eps)
def _scatter_add(tgt, idx, src, axis=0):
    return tgt.clone().index_add_(axis, idx, src)
def _segment_sum(src, seg, n):
    out = torch.zeros((n,) + src.shape[1:], dtype=src.dtype)
    return out.index_add_(0, seg, src)
def _pad(x, pw):
    flat = tuple(v for pair in reversed(pw) for v in pair)
    return torch.nn.functional.pad(x, flat)

kernel_name = 'parallel_sum__ssm_conv_s4d__glu_gated_reglu__prompt'

def setup_inputs():

    _w = (lambda a: (lambda t: t.to(torch.bfloat16) if t.is_floating_point() else t)(torch.as_tensor(a)))
    def _A_setup():
        Bsz, L, D, N = 1, 2048, 512, 128
        return {
            "u": _w((lambda *s: torch.randn(*s, dtype=torch.bfloat16) * 0.05)(Bsz, L, D)),
            "log_A_real": _w((lambda *s: torch.randn(*s, dtype=torch.bfloat16) * 3.2)(D, N) * 0.5),
            "A_imag": _w((lambda *s: torch.randn(*s, dtype=torch.bfloat16) * 0.05)(D, N) * 3.14),
            "C_real": _w((lambda *s: torch.randn(*s, dtype=torch.bfloat16) * 0.1)(D, N) / N ** 0.5),
            "C_imag": _w((lambda *s: torch.randn(*s, dtype=torch.bfloat16) * 0.2)(D, N) / N ** 0.5),
            "log_dt": _w((lambda *s: torch.randn(*s, dtype=torch.bfloat16) * 0.05)(D) * 0.5 - 4.0),
            "D_skip": _w((lambda *s: torch.randn(*s, dtype=torch.bfloat16) * 0.05)(D)),
        }
    _A_in = _A_setup()

    def _B_setup():
        B, S, D, F = 1, 2048, 512, 1376
        return {
            "x": _w((lambda *s: torch.randn(*s, dtype=torch.bfloat16) * 0.05)(B, S, D)),
            "W_gate": _w((lambda *s: torch.randn(*s, dtype=torch.bfloat16) * 0.4)(D, F) * (2.0 / D) ** 0.5),
            "W_up": _w((lambda *s: torch.randn(*s, dtype=torch.bfloat16) * 0.4)(D, F) * (2.0 / D) ** 0.5),
            "W_down": _w((lambda *s: torch.randn(*s, dtype=torch.bfloat16) * 0.4)(F, D) * (1.0 / F) ** 0.5),
        }
    _B_in = _B_setup()

    return {
        "A_u": _A_in["u"],
        "A_log_A_real": _A_in["log_A_real"],
        "A_A_imag": _A_in["A_imag"],
        "A_C_real": _A_in["C_real"],
        "A_C_imag": _A_in["C_imag"],
        "A_log_dt": _A_in["log_dt"],
        "A_D_skip": _A_in["D_skip"],
        "B_x": _B_in["x"],
        "B_W_gate": _B_in["W_gate"],
        "B_W_up": _B_in["W_up"],
        "B_W_down": _B_in["W_down"],
    }

def reference(A_u, A_log_A_real, A_A_imag, A_C_real, A_C_imag, A_log_dt, A_D_skip, B_x, B_W_gate, B_W_up, B_W_down):
    def _A_ref(u, log_A_real, A_imag, C_real, C_imag, log_dt, D_skip):
        L = u.shape[1]
        dt = torch.exp(log_dt)
        A = -torch.exp(log_A_real) + 1j * A_imag
        C = (C_real + 1j * C_imag) * dt[:, None]
        pos = (lambda x, dt: x.to(dt))(torch.arange(L), log_dt.dtype)
        P = torch.exp((A * dt[:, None])[:, :, None] * pos[None, None, :])
        K = 2.0 * torch.einsum("dn,dnl->dl", C, P).real
        L2 = 2 * L
        u_t = (lambda x, *d: x.permute(*d))(u, 0, 2, 1)
        Kf = (lambda x, *a, **k: torch.fft.rfft(x.float(), *a, **k))(K, L2)
        Uf = (lambda x, *a, **k: torch.fft.rfft(x.float(), *a, **k))(u_t, L2)
        y = (lambda x, *a, **k: torch.fft.irfft(x, *a, **k).to(torch.bfloat16))(Uf * Kf[None, :, :], L2)[:, :, :L]
        y = (lambda x, *d: x.permute(*d))(y, 0, 2, 1)
        return y + u * D_skip[None, None, :]

    def _B_ref(x, W_gate, W_up, W_down):
        gate = torch.einsum("bsd,df->bsf", x, W_gate)
        up = torch.einsum("bsd,df->bsf", x, W_up)
        gate = gate * (lambda x, dt: x.to(dt))(gate > 0, gate.dtype)
        h = gate * up
        return torch.einsum("bsf,fd->bsd", h, W_down)

    x = A_u
    xn = _layernorm(x, axis=-1)
    return x + _A_ref(xn, A_log_A_real, A_A_imag, A_C_real, A_C_imag, A_log_dt, A_D_skip) + _B_ref(xn, B_W_gate, B_W_up, B_W_down)
```

```cpp
#include <hip/hip_runtime.h>
#include <hip/hip_bf16.h>
#include <stdint.h>

typedef unsigned short u16;
constexpr int L = 2048, D = 512, N = 128, F = 1376;

__device__ __forceinline__ float bf2f(u16 b) { return __uint_as_float(((unsigned)b) << 16); }
__device__ __forceinline__ u16 f2bf(float f) { __hip_bfloat16 h = __float2bfloat16(f); return *(u16*)&h; }
__device__ __forceinline__ float rbf(float f) { return bf2f(f2bf(f)); }

__global__ void k_ln(const u16* x, u16* xn) {
  int row = blockIdx.x, t = threadIdx.x;
  float v[8]; float s = 0;
  for (int j = 0; j < 8; ++j) { v[j] = bf2f(x[row * D + t * 8 + j]); s += v[j]; }
  for (int o = 32; o; o >>= 1) s += __shfl_xor(s, o);
  float mean = s * (1.0f / D);
  float q = 0;
  for (int j = 0; j < 8; ++j) { float d = v[j] - mean; q += d * d; }
  for (int o = 32; o; o >>= 1) q += __shfl_xor(q, o);
  float var = rbf(q * (1.0f / D));
  float m = rbf(mean);
  float sd = rbf(sqrtf(rbf(var + 1e-5f)));
  for (int j = 0; j < 8; ++j) xn[row * D + t * 8 + j] = f2bf(rbf(v[j] - m) / sd);
}

__global__ void k_kgen(const u16* lar, const u16* aim, const u16* cr, const u16* ci, const u16* ldt, float* K) {
  int d = blockIdx.x;
  __shared__ float ar[N], ai[N], c_r[N], c_i[N];
  float dt = rbf(expf(bf2f(ldt[d])));
  for (int n = threadIdx.x; n < N; n += blockDim.x) {
    ar[n] = -rbf(expf(bf2f(lar[d * N + n]))) * dt;
    ai[n] = bf2f(aim[d * N + n]) * dt;
    c_r[n] = bf2f(cr[d * N + n]) * dt;
    c_i[n] = bf2f(ci[d * N + n]) * dt;
  }
  __syncthreads();
  for (int l = threadIdx.x; l < L; l += blockDim.x) {
    float p = rbf((float)l);
    float acc = 0;
    for (int n = 0; n < N; ++n) {
      float e = expf(ar[n] * p);
      float s, c; sincosf(ai[n] * p, &s, &c);
      acc += e * (c_r[n] * c - c_i[n] * s);
    }
    K[d * L + l] = 2.0f * acc;
  }
}

__global__ void k_conv(const u16* xn, const float* K, const u16* dsk, u16* ya) {
  int d = blockIdx.x;
  __shared__ float sk[L], su[L];
  for (int l = threadIdx.x; l < L; l += blockDim.x) { sk[l] = K[d * L + l]; su[l] = bf2f(xn[l * D + d]); }
  __syncthreads();
  float ds = bf2f(dsk[d]);
  for (int l = threadIdx.x; l < L; l += blockDim.x) {
    float acc = 0;
    for (int j = 0; j <= l; ++j) acc += sk[j] * su[l - j];
    ya[l * D + d] = f2bf(rbf(acc) + rbf(su[l] * ds));
  }
}

__global__ void k_gemm1(const u16* xn, const u16* wg, const u16* wu, u16* h) {
  int row = blockIdx.x;
  __shared__ float sx[D];
  for (int k = threadIdx.x; k < D; k += blockDim.x) sx[k] = bf2f(xn[row * D + k]);
  __syncthreads();
  for (int f = threadIdx.x; f < F; f += blockDim.x) {
    float g = 0, u = 0;
    for (int k = 0; k < D; ++k) { g += sx[k] * bf2f(wg[k * F + f]); u += sx[k] * bf2f(wu[k * F + f]); }
    g = rbf(g); u = rbf(u);
    g = g > 0 ? g : 0.f * g;
    h[row * F + f] = f2bf(g * u);
  }
}

__global__ void k_gemm2(const u16* h, const u16* wd, const u16* x, const u16* ya, u16* out) {
  int row = blockIdx.x;
  __shared__ float sh[F];
  for (int k = threadIdx.x; k < F; k += blockDim.x) sh[k] = bf2f(h[row * F + k]);
  __syncthreads();
  for (int d = threadIdx.x; d < D; d += blockDim.x) {
    float o = 0;
    for (int k = 0; k < F; ++k) o += sh[k] * bf2f(wd[k * D + d]);
    float r = rbf(bf2f(x[row * D + d]) + bf2f(ya[row * D + d]));
    out[row * D + d] = f2bf(r + rbf(o));
  }
}

extern "C" void kernel_launch(void* const* d_in, const int* in_sizes, int n_in,
                              void* d_out, int out_size, void* d_ws, size_t ws_size,
                              hipStream_t stream) {
  const u16* x = (const u16*)d_in[0];
  char* ws = (char*)d_ws;
  u16* xn = (u16*)ws;
  float* K = (float*)(ws + (4 << 20));
  u16* ya = (u16*)(ws + (8 << 20));
  u16* h = (u16*)(ws + (12 << 20));
  k_ln<<<L, 64, 0, stream>>>(x, xn);
  k_kgen<<<D, 256, 0, stream>>>((const u16*)d_in[1], (const u16*)d_in[2], (const u16*)d_in[3], (const u16*)d_in[4], (const u16*)d_in[5], K);
  k_conv<<<D, 256, 0, stream>>>(xn, K, (const u16*)d_in[6], ya);
  k_gemm1<<<L, 256, 0, stream>>>(xn, (const u16*)d_in[8], (const u16*)d_in[9], h);
  k_gemm2<<<L, 256, 0, stream>>>(h, (const u16*)d_in[10], x, ya, (u16*)d_out);
}
```

```cpp
#define POLL_SLEEP 4
#define ROT (((cb + rb) & 1) * 4)
#include <hip/hip_runtime.h>
#include <hip/hip_bf16.h>
#include <stdint.h>

typedef unsigned short u16;
typedef unsigned v4u __attribute__((ext_vector_type(4)));
typedef unsigned v2u __attribute__((ext_vector_type(2)));
typedef short v4s __attribute__((ext_vector_type(4)));
typedef float f32x2 __attribute__((ext_vector_type(2)));
typedef float f32x4 __attribute__((ext_vector_type(4)));
typedef float f32x16 __attribute__((ext_vector_type(16)));
typedef __bf16 bf16x2 __attribute__((ext_vector_type(2)));
typedef __bf16 bf16x8 __attribute__((ext_vector_type(8)));
typedef _Float16 f16x2 __attribute__((ext_vector_type(2)));
typedef _Float16 f16x8 __attribute__((ext_vector_type(8)));
#define LAS __attribute__((address_space(3)))
#if defined(__HIP_DEVICE_COMPILE__)
#define NOPK __attribute__((target("no-packed-fp32-ops")))
#else
#define NOPK
#endif
#define DEVI NOPK __device__ __forceinline__

constexpr size_t WS_E = 0;
constexpr size_t WS_F3 = 4096;
constexpr size_t WS_STAT = 8192;
constexpr size_t WS_CS = 32768;
constexpr size_t WS_XG = 65536;
constexpr size_t WS_YG = WS_XG + (4u << 20);
constexpr size_t WS_H = WS_YG + (4u << 20);

constexpr int SLOT = 40960;
constexpr int L_U = 114688, UCH = 4576;
constexpr int L_KC = 123904, KCP = 4160, KCCH = 4 * KCP;
constexpr int KGP = 528, LNP = 1088, LNOFF = 8448;
constexpr int L_TBL = 157184, TBLCH = 1568;
constexpr int L_CSX = 160320;
constexpr int L_R2 = 81920, L_R0 = 0, L_R1 = 40960;
constexpr int L_RED = 114688, REDP = 272, REDQ = 64 * REDP;
constexpr int L_AIMG = 149504, L_RS2 = 157696;
constexpr int L_NM = 163392;
constexpr int L_SYNC = 163712;
constexpr int S_FULL = L_SYNC, S_LN = L_SYNC + 4, S_KRDY = L_SYNC + 8, S_URDY = L_SYNC + 12, S_FREE = L_SYNC + 16,
              S_HCNT = L_SYNC + 32, S_PRE = L_SYNC + 44, S_CONV = L_SYNC + 48, S_AIMG = L_SYNC + 52, S_TAG = L_SYNC + 56, S_CS = L_SYNC + 60, S_STD = L_SYNC + 36, S_HRDY = L_SYNC + 40, S_PAIR = L_SYNC + 64, S_REC = L_SYNC + 72;
constexpr int LDS_BYTES = 163840;

constexpr unsigned SPIN_CAP = 1u << 16, LSPIN_CAP = 1u << 22;

#define BARRIER() asm volatile("s_waitcnt lgkmcnt(0)\n\ts_barrier" ::: "memory")
#define WAITV(n) asm volatile("s_waitcnt vmcnt(" #n ")" ::: "memory")
#define SCHED() __builtin_amdgcn_sched_barrier(0)
#define MEMF() asm volatile("" ::: "memory")

DEVI void glds4(unsigned v0, unsigned v1, unsigned v2, unsigned v3, const void* sb, unsigned lds) {
  asm volatile("s_mov_b32 m0, %5\n\ts_nop 0\n\tglobal_load_lds_dwordx4 %0, %4\n\tglobal_load_lds_dwordx4 %1, %4 offset:1024\n\t"
               "global_load_lds_dwordx4 %2, %4 offset:2048\n\tglobal_load_lds_dwordx4 %3, %4 offset:3072"
               :: "v"(v0), "v"(v1), "v"(v2), "v"(v3), "s"(sb), "s"(lds) : "memory");
}
DEVI void glds3(unsigned v0, unsigned v1, unsigned v2, const void* sb, unsigned lds) {
  asm volatile("s_mov_b32 m0, %4\n\ts_nop 0\n\tglobal_load_lds_dwordx4 %0, %3\n\tglobal_load_lds_dwordx4 %1, %3 offset:1024\n\t"
               "global_load_lds_dwordx4 %2, %3 offset:2048"
               :: "v"(v0), "v"(v1), "v"(v2), "s"(sb), "s"(lds) : "memory");
}
DEVI void glds1(unsigned v0, const void* sb, unsigned lds) {
  asm volatile("s_mov_b32 m0, %2\n\ts_nop 0\n\tglobal_load_lds_dwordx4 %0, %1" :: "v"(v0), "s"(sb), "s"(lds) : "memory");
}
DEVI void glds2(unsigned v0, unsigned v1, const void* sb, unsigned lds) {
  asm volatile("s_mov_b32 m0, %3\n\ts_nop 0\n\tglobal_load_lds_dwordx4 %0, %2\n\tglobal_load_lds_dwordx4 %1, %2 offset:1024"
               :: "v"(v0), "v"(v1), "s"(sb), "s"(lds) : "memory");
}
DEVI void ld16_sc1(v4u& d, unsigned voff, const void* sbase) {
  asm volatile("global_load_dwordx4 %0, %1, %2 sc1" : "=v"(d) : "v"(voff), "s"(sbase) : "memory");
}
DEVI void ld16(v4u& d, unsigned voff, const void* sbase) {
  asm volatile("global_load_dwordx4 %0, %1, %2" : "=v"(d) : "v"(voff), "s"(sbase) : "memory");
}
DEVI void ld8_sc1(v2u& d, unsigned voff, const void* sbase) {
  asm volatile("global_load_dwordx2 %0, %1, %2 sc1" : "=v"(d) : "v"(voff), "s"(sbase) : "memory");
}
DEVI void ld4_sc1(unsigned& d, unsigned voff, const void* sbase) {
  asm volatile("global_load_dword %0, %1, %2 sc1" : "=v"(d) : "v"(voff), "s"(sbase) : "memory");
}
DEVI void ld4(unsigned& d, unsigned voff, const void* sbase) {
  asm volatile("global_load_dword %0, %1, %2" : "=v"(d) : "v"(voff), "s"(sbase) : "memory");
}
DEVI void st16_sc1(v4u d, unsigned voff, void* sbase) {
  asm volatile("global_store_dwordx4 %0, %1, %2 sc1\n\ts_nop 1" :: "v"(voff), "v"(d), "s"(sbase) : "memory");
}
DEVI void st16(v4u d, unsigned voff, void* sbase) {
  asm volatile("global_store_dwordx4 %0, %1, %2\n\ts_nop 1" :: "v"(voff), "v"(d), "s"(sbase) : "memory");
}
DEVI void st8_sc1(v2u d, unsigned voff, void* sbase) {
  asm volatile("global_store_dwordx2 %0, %1, %2 sc1" :: "v"(voff), "v"(d), "s"(sbase) : "memory");
}
DEVI void st4_sc1(unsigned d, unsigned voff, void* sbase) {
  asm volatile("global_store_dword %0, %1, %2 sc1" :: "v"(voff), "v"(d), "s"(sbase) : "memory");
}
#define PIN(x) asm volatile("" : "+v"(x))

DEVI float bflo(unsigned w) { return __builtin_bit_cast(float, w << 16); }
DEVI float bfhi(unsigned w) { return __builtin_bit_cast(float, w & 0xffff0000u); }
DEVI unsigned pkbf(float a, float b) { f32x2 f = {a, b}; return __builtin_bit_cast(unsigned, __builtin_convertvector(f, bf16x2)); }
DEVI unsigned pkh(float a, float b) { f32x2 f = {a, b}; return __builtin_bit_cast(unsigned, __builtin_convertvector(f, f16x2)); }
DEVI float rbf(float a) { return bflo(pkbf(a, a)); }
DEVI int imax(int a, int b) { return a > b ? a : b; }
DEVI int imin(int a, int b) { return a < b ? a : b; }
template <int CTRL> DEVI float dppf(float v) {
  return __builtin_bit_cast(float, __builtin_amdgcn_update_dpp(0, __builtin_bit_cast(int, v), CTRL, 0xf, 0xf, true));
}
DEVI float sum8(float v) { v += dppf<0xB1>(v); v += dppf<0x4E>(v); v += dppf<0x141>(v); return v; }
DEVI int wave_maxi(int v) {
  v = imax(v, __builtin_amdgcn_update_dpp(0, v, 0x111, 0xf, 0xf, true));
  v = imax(v, __builtin_amdgcn_update_dpp(0, v, 0x112, 0xf, 0xf, true));
  v = imax(v, __builtin_amdgcn_update_dpp(0, v, 0x114, 0xf, 0xf, true));
  v = imax(v, __builtin_amdgcn_update_dpp(0, v, 0x118, 0xf, 0xf, true));
  v = imax(v, __builtin_amdgcn_update_dpp(0, v, 0x142, 0xa, 0xf, true));
  v = imax(v, __builtin_amdgcn_update_dpp(0, v, 0x143, 0xc, 0xf, true));
  return __builtin_amdgcn_readlane(v, 63);
}
DEVI float expacc(float x) {
  const float L = 1.44269504088896341f, Ll = 1.925963033500011e-8f;
  float hi = x * L, lo = __builtin_fmaf(x, L, -hi) + x * Ll;
  float e = __builtin_amdgcn_exp2f(hi);
  return __builtin_fmaf(e, lo * 0.6931471805599453f, e);
}
struct cf { float r, i; };
DEVI cf cmul(cf a, cf b) { cf c; c.r = a.r * b.r - a.i * b.i; c.i = a.r * b.i + a.i * b.r; return c; }
DEVI cf cexpp(float ar2, float ai2, float p) {
  float e = __builtin_amdgcn_exp2f(ar2 * p);
  float r = __builtin_amdgcn_fractf(ai2 * p);
  cf c; c.r = e * __builtin_amdgcn_cosf(r); c.i = e * __builtin_amdgcn_sinf(r); return c;
}
DEVI f32x16 mfma32(v4u a, v4u b, f32x16 c) {
  return __builtin_amdgcn_mfma_f32_32x32x16_bf16(__builtin_bit_cast(bf16x8, a), __builtin_bit_cast(bf16x8, b), c, 0, 0, 0);
}
DEVI f32x4 mfma16(v4u a, v4u b, f32x4 c) {
  return __builtin_amdgcn_mfma_f32_16x16x32_bf16(__builtin_bit_cast(bf16x8, a), __builtin_bit_cast(bf16x8, b), c, 0, 0, 0);
}
DEVI f32x4 mfma16h(v4u a, v4u b, f32x4 c) {
  return __builtin_amdgcn_mfma_f32_16x16x32_f16(__builtin_bit_cast(f16x8, a), __builtin_bit_cast(f16x8, b), c, 0, 0, 0);
}
DEVI float dot2ones(unsigned w, float acc) {
  bf16x2 ones = {(__bf16)1.0f, (__bf16)1.0f};
  return __builtin_amdgcn_fdot2_f32_bf16(__builtin_bit_cast(bf16x2, w), ones, acc, false);
}
DEVI bool allv(bool p) { return __builtin_amdgcn_ballot_w64(p) == ~0ull; }

#define STAMP(i)

#define LD128(a) (*(LAS const v4u*)(sm + (a)))
#define LD64V(a) (*(LAS const volatile v2u*)(sm + (a)))
#define LDTR(a) __builtin_bit_cast(v2u, __builtin_amdgcn_ds_read_tr16_b64_v4i16((LAS v4s*)(sm + (a))))
#define ST128(a, v) (*(LAS v4u*)(sm + (a)) = (v))
#define ST64(a, v) (*(LAS v2u*)(sm + (a)) = (v))
#define ST16(a, v) (*(LAS u16*)(sm + (a)) = (u16)(v))
#define LD16(a) ((unsigned)*(LAS const u16*)(sm + (a)))
DEVI v4u trfrag(LAS char* sm, int a, int d) { v2u p = LDTR(a), q = LDTR(a + d); return v4u{p.x, p.y, q.x, q.y}; }
DEVI unsigned lds_rd(LAS char* sm, int a) { return __builtin_amdgcn_readfirstlane(*(LAS volatile unsigned*)(sm + a)); }
DEVI unsigned spin_ge(unsigned vaddr, unsigned n) {
  unsigned cnt, tmp, cur;
  asm volatile("s_mov_b32 %1, 0x400000\n"
               "1:\n\tds_read_b32 %2, %3\n\ts_waitcnt lgkmcnt(0)\n\tv_readfirstlane_b32 %0, %2\n\ts_sub_u32 %1, %1, 1\n\t"
               "s_cbranch_scc1 2f\n\ts_cmp_lt_u32 %0, %4\n\ts_cbranch_scc1 1b\n2:"
               : "=&s"(cur), "=&s"(cnt), "=&v"(tmp) : "v"(vaddr), "s"(__builtin_amdgcn_readfirstlane(n)) : "memory", "scc");
  return __builtin_amdgcn_readfirstlane(cur);
}
DEVI void wait_ge(LAS char* sm, int a, unsigned n) {
  unsigned cnt, tmp, cur;
  asm volatile("s_mov_b32 %1, 0x400000\n"
               "1:\n\tds_read_b32 %2, %3\n\ts_waitcnt lgkmcnt(0)\n\tv_readfirstlane_b32 %0, %2\n\ts_cmp_ge_u32 %0, %4\n\t"
               "s_cbranch_scc1 2f\n\ts_sleep 1\n\ts_sub_u32 %1, %1, 1\n\ts_cbranch_scc0 1b\n2:"
               : "=&s"(cur), "=&s"(cnt), "=&v"(tmp) : "v"((unsigned)(uintptr_t)(sm + a)), "s"(n) : "memory", "scc");
}
DEVI f32x4 lanesum4(unsigned a, unsigned b, f32x4 c) {
  const v4s ones = {0x3f80, 0x3f80, 0x3f80, 0x3f80};
  return __builtin_amdgcn_mfma_f32_4x4x4bf16_1k(ones, __builtin_bit_cast(v4s, (v2u{a, b})), c, 0, 0, 0);
}
DEVI void wait_free(LAS char* sm, unsigned n) {
#pragma unroll 1
  for (unsigned i = 0; i < LSPIN_CAP; ++i) {
    v4u f = *(LAS volatile v4u*)(sm + S_FREE);
    unsigned m = f.x < f.y ? f.x : f.y, m2 = f.z < f.w ? f.z : f.w;
    if (__builtin_amdgcn_readfirstlane(m < m2 ? m : m2) >= n) break;
    __builtin_amdgcn_s_sleep(1);
  }
  MEMF();
}
DEVI void sig_set(LAS char* sm, int a, unsigned v) {
  asm volatile("s_mov_b64 exec, 1\n\tds_write_b32 %0, %1\n\ts_mov_b64 exec, -1" :: "v"((unsigned)(uintptr_t)(sm + a)), "v"(v) : "memory");
}
DEVI void sig_add(LAS char* sm, int a, int lane) {
  asm volatile("s_mov_b64 exec, 1\n\tds_add_u32 %0, %1\n\ts_mov_b64 exec, -1" :: "v"((unsigned)(uintptr_t)(sm + a)), "v"(1u) : "memory");
}
DEVI int wd_slot(int kh, int j) { return j < 8 ? L_R2 + (8 * kh + j) * 2048 : j < 18 ? L_R0 + (10 * kh + j - 8) * 2048 : L_R1 + (10 * kh + j - 18) * 2048; }

NOPK __global__ __launch_bounds__(512, 2) void fused_kernel(
    const u16* __restrict__ x, const u16* __restrict__ lar, const u16* __restrict__ aim, const u16* __restrict__ cre,
    const u16* __restrict__ cim, const u16* __restrict__ ldt, const u16* __restrict__ dsk, const u16* __restrict__ wg,
    const u16* __restrict__ wu, const u16* __restrict__ wd, u16* __restrict__ out, char* __restrict__ ws) {
  __shared__ __attribute__((aligned(16))) char smem[LDS_BYTES];
  LAS char* const sm = (LAS char*)smem;
  const unsigned lb = (unsigned)(uintptr_t)sm;
  const int bid = __builtin_amdgcn_workgroup_id_x(), tid = __builtin_amdgcn_workitem_id_x(), lane = tid & 63;
  const int w = __builtin_amdgcn_readfirstlane(tid >> 6);
  const int xcd = bid & 7, idx = bid >> 3;
  const bool csrole = idx >= 30;
  const int rb = (xcd >> 1) * 4 + (idx < 28 ? idx / 7 : (xcd & 1) * 2 + (idx & 1));
  const int cb = idx < 28 ? (xcd & 1) * 8 + idx % 7 : (idx < 30 ? 7 : imin((xcd & 1) * 8 + (xcd >> 1) * 2 + (idx & 1), 14));
  const int col0 = imin(cb * 96, 1280);
  const int cp = xcd * 32 + idx;
  const int tm = xcd * 4 + (idx >> 3), tn = idx & 7;
  const int hh = lane >> 5, l31 = lane & 31, l15 = lane & 15, g4 = lane >> 4;
  const unsigned oo = (unsigned)((tid >> 3) * 1024 + (tid & 7) * 16);

  if (w == 7) {
    __builtin_amdgcn_s_setprio(3);
    unsigned xv[4], gv[3], uv[3], dv[4];
#pragma unroll
    for (int j = 0; j < 4; ++j) {
      xv[j] = (lane >> 3) * 1024 + (((lane & 7) ^ ((4 * j + g4) & 7)) << 4) + j * 7168;
      dv[j] = (lane >> 3) * 1024 + (((lane & 7) ^ ((g4 & 1) << 2)) << 4) + j * 7168;
    }
#pragma unroll
    for (int j = 0; j < 3; ++j) {
      int p = 16 * j + (lane >> 2);
      gv[j] = (p / 3) * 2752 + (p % 3) * 64 + (lane & 3) * 16 - j * 1024;
      uv[j] = gv[j] ^ 32;
    }
    const char* xbase = (const char*)(x + (size_t)rb * 128 * 512);
    const char* gbase = (const char*)wg + col0 * 2;
    const char* ubase = (const char*)wu + col0 * 2;
    const int rot = ROT;
    unsigned slot = lb;
    const char* dbase = (const char*)wd + tn * 128;
#pragma unroll 1
    for (int s = 0; s < 8; ++s) {
      if (s == 1) BARRIER();
      if (s >= 3) wait_free(sm, s - 2);
      const int ks = (s + rot) & 7;
      const char* xb = xbase + ks * 128;
      const char* gb = gbase + ks * (64 * 2752);
      const char* ub = ubase + ks * (64 * 2752);
      if (!csrole) {
#pragma unroll
        for (int i = 0; i < 4; ++i) glds4(xv[0], xv[1], xv[2], xv[3], xb + i * 32768, slot + i * 4096);
      }
#pragma unroll
      for (int k = 0; k < 4; ++k) {
        glds3(gv[0], gv[1], gv[2], gb + k * (16 * 2752), slot + 16384 + k * 3072);
        glds3(uv[0], uv[1], uv[2], ub + k * (16 * 2752), slot + 28672 + k * 3072);
        if (s >= 1) {
          if (csrole) { WAITV(36); sig_set(sm, S_FULL, imax(4 * s + k - 5, 0)); }
          else { WAITV(40); sig_set(sm, S_FULL, 4 * (s - 1) + k + 1); }
        }
      }
      slot += SLOT; if (slot == lb + 3 * SLOT) slot = lb;
    }
    STAMP(24);
    if (csrole) { WAITV(0); sig_set(sm, S_FULL, 32); }
    wait_free(sm, 6);
    const unsigned f0 = csrole ? 32 : 29, fs = csrole ? 0 : 1;
    glds4(dv[0], dv[1], dv[2], dv[3], dbase, lb + wd_slot(0, 0)); glds4(dv[0], dv[1], dv[2], dv[3], dbase + 43 * 16384, lb + wd_slot(1, 0));
    WAITV(26); sig_set(sm, S_FULL, f0);
    glds4(dv[0], dv[1], dv[2], dv[3], dbase + 2 * 16384, lb + wd_slot(0, 2)); glds4(dv[0], dv[1], dv[2], dv[3], dbase + 45 * 16384, lb + wd_slot(1, 2));
    WAITV(28); sig_set(sm, S_FULL, f0 + fs);
    glds4(dv[0], dv[1], dv[2], dv[3], dbase + 4 * 16384, lb + wd_slot(0, 4)); glds4(dv[0], dv[1], dv[2], dv[3], dbase + 47 * 16384, lb + wd_slot(1, 4));
    WAITV(30); sig_set(sm, S_FULL, f0 + 2 * fs);
    glds4(dv[0], dv[1], dv[2], dv[3], dbase + 6 * 16384, lb + wd_slot(0, 6)); glds4(dv[0], dv[1], dv[2], dv[3], dbase + 49 * 16384, lb + wd_slot(1, 6));
    WAITV(32); sig_set(sm, S_FULL, 32);
    STAMP(25);
    wait_free(sm, 7);
#pragma unroll 1
    for (int j = 0; j < 10; j += 2) {
      glds4(dv[0], dv[1], dv[2], dv[3], dbase + (8 + j) * 16384, lb + L_R0 + j * 2048);
      glds4(dv[0], dv[1], dv[2], dv[3], dbase + (51 + j) * 16384, lb + L_R0 + (10 + j) * 2048);
    }
    WAITV(0); sig_set(sm, S_FULL, 33);
    STAMP(26);
    wait_ge(sm, S_STD, 4);
#pragma unroll 1
    for (int j = 0; j < 10; j += 2) {
      glds4(dv[0], dv[1], dv[2], dv[3], dbase + (18 + j) * 16384, lb + L_R1 + j * 2048);
      glds4(dv[0], dv[1], dv[2], dv[3], dbase + (61 + j) * 16384, lb + L_R1 + (10 + j) * 2048);
    }
    WAITV(0); sig_set(sm, S_FULL, 34);
    STAMP(27);
    __builtin_amdgcn_s_setprio(0);
  }
  unsigned tag;

  if (w < 4) {
    __builtin_amdgcn_s_setprio(2);
    if (w == 0 && lane < 32) *(LAS volatile unsigned*)(sm + L_SYNC + lane * 4) = 0u;
    BARRIER();
    const int tb2 = w & 1, chf = w >> 1;
    int xa[4], wa[3];
    {
      int tok = 64 * tb2 + l31;
      int b0 = tok * 128 + ((hh ^ ((tok >> 1) & 7)) << 4);
#pragma unroll
      for (int s = 0; s < 4; ++s) xa[s] = b0 ^ (s << 5);
      int mat = g4 & 1, q = (lane >> 2) & 3, p = lane & 3;
#pragma unroll
      for (int t = 0; t < 3; ++t) {
        int T = 3 * chf + t;
        wa[t] = 16384 + mat * 12288 + (8 * hh + q) * 192 + (T >> 1) * 64 + (((T & 1) ^ mat) << 5) + p * 8;
      }
    }
    const unsigned fulladdr = lb + S_FULL;
    if (csrole) {
      f32x4 cs[3];
#pragma unroll
      for (int t = 0; t < 3; ++t) { cs[t] = f32x4{0.f, 0.f, 0.f, 0.f}; wa[t] += tb2 * 768; }
#pragma unroll 1
      for (int s = 0; s < 8; ++s) {
        wait_ge(sm, S_FULL, 4 * s + 4);
        v2u f[4][3];
#pragma unroll
        for (int k = 0; k < 4; ++k)
#pragma unroll
          for (int t = 0; t < 3; ++t) f[k][t] = LDTR(wa[t] + k * 3072);
        sig_set(sm, S_FREE + w * 4, s + 1);
#pragma unroll
        for (int k = 0; k < 4; ++k)
#pragma unroll
          for (int t = 0; t < 3; ++t) cs[t] = lanesum4(f[k][t].x, f[k][t].y, cs[t]);
        const int adv = (s == 2 || s == 5) ? -2 * SLOT : SLOT;
#pragma unroll
        for (int t = 0; t < 3; ++t) wa[t] += adv;
      }
#pragma unroll
      for (int t = 0; t < 3; ++t) *(LAS float*)(sm + L_CSX + ((w * 3 + t) * 64 + lane) * 4) = cs[t][0];
      sig_add(sm, S_HCNT, lane);
      wait_ge(sm, S_LN, 1);
      tag = lds_rd(sm, S_TAG);
      if (w < 3) {
        wait_ge(sm, S_HCNT, 4);
        const int T = 2 * w + hh, c2 = T >= 3 ? 1 : 0, t = T - 3 * c2;
        const LAS float* p = (const LAS float*)(sm + L_CSX) + ((2 * c2) * 3 + t) * 64 + l31;
        float c = (p[0] + p[32]) + (p[192] + p[224]);
        st8_sc1(v2u{__builtin_bit_cast(unsigned, c), tag}, (unsigned)((64 * w + lane) * 8), ws + WS_CS + cb * 2048);
      }
      STAMP(2);
      sig_add(sm, S_STD, lane);
    } else if (cb == 14 && chf == 0) {
      sig_set(sm, S_FREE + w * 4, 8);
      sig_add(sm, S_STD, lane);
      wait_ge(sm, S_LN, 1);
      tag = lds_rd(sm, S_TAG);
    } else {
      f32x16 acc[2][3];
#pragma unroll
      for (int b = 0; b < 2; ++b)
#pragma unroll
        for (int t = 0; t < 3; ++t)
#pragma unroll
          for (int r = 0; r < 16; ++r) acc[b][t][r] = 0.f;
      v4u x0[2], w0[3], x1[2], w1[3], x2[2], w2[3], x3[2], w3[3];
      unsigned fv;
#define G1_M(X, W, b, t) do { SCHED(); acc[b][t] = mfma32(W[t], X[b], acc[b][t]); SCHED(); } while (0)
#define G1_STEP(Xc, Wc, Xn, Wn, kn, need, EXTRA) do { \
        G1_M(Xc, Wc, 0, 0); \
        const unsigned fs_ = __builtin_amdgcn_readfirstlane(fv); \
        G1_M(Xc, Wc, 0, 1); \
        if (__builtin_expect(fs_ < (unsigned)(need), 0)) spin_ge(fulladdr, (need)); \
        MEMF(); Xn[0] = LD128(xa[kn]); \
        G1_M(Xc, Wc, 0, 2); \
        Wn[0] = trfrag(sm, wa[0] + (kn) * 3072, 768); \
        G1_M(Xc, Wc, 1, 0); \
        fv = *(LAS volatile unsigned*)(sm + S_FULL); \
        Wn[1] = trfrag(sm, wa[1] + (kn) * 3072, 768); \
        G1_M(Xc, Wc, 1, 1); \
        Wn[2] = trfrag(sm, wa[2] + (kn) * 3072, 768); \
        G1_M(Xc, Wc, 1, 2); \
        Xn[1] = LD128(xa[kn] + 4096); \
        xa[kn] += adv; \
        EXTRA; \
      } while (0)
      fv = spin_ge(fulladdr, 2);
      STAMP(1);
      x0[0] = LD128(xa[0]); x0[1] = LD128(xa[0] + 4096); x1[0] = LD128(xa[1]); x1[1] = LD128(xa[1] + 4096);
#pragma unroll
      for (int t = 0; t < 3; ++t) { w0[t] = trfrag(sm, wa[t], 768); w1[t] = trfrag(sm, wa[t] + 3072, 768); }
      xa[0] += SLOT; xa[1] += SLOT;
#pragma unroll 1
      for (int s = 0; s < 8; ++s) {
        int adv = (s == 2 || s == 5) ? -2 * SLOT : SLOT;
        G1_STEP(x0, w0, x2, w2, 2, 4 * s + 3, );
        G1_STEP(x1, w1, x3, w3, 3, 4 * s + 4, { sig_set(sm, S_FREE + w * 4, s + 1); wa[0] += adv; wa[1] += adv; wa[2] += adv; });
        adv = (s == 1 || s == 4) ? -2 * SLOT : SLOT;
        G1_STEP(x2, w2, x0, w0, 0, imin(4 * s + 5, 32), );
        G1_STEP(x3, w3, x1, w1, 1, imin(4 * s + 6, 32), );
      }
      STAMP(2);
      wait_ge(sm, S_CS, 1);
      tag = lds_rd(sm, S_TAG);
      STAMP(3);
      v4u am[3];
#pragma unroll
      for (int t = 0; t < 3; ++t) {
        v2u a = *(LAS const v2u*)(sm + L_CSX + ((3 * chf + t) * 32 + l31) * 8);
        am[t] = v4u{hh ? 0u : a.x, hh ? 0u : a.y, 0u, 0u};
      }
#pragma unroll
      for (int t = 0; t < 3; ++t)
#pragma unroll
        for (int b = 0; b < 2; ++b) {
          const unsigned nm = LD16(L_NM + (64 * tb2 + 32 * b + l31) * 2);
          acc[b][t] = mfma32(am[t], v4u{nm | (nm << 16), nm, 0u, 0u}, acc[b][t]);
        }
      char* hb = ws + WS_H + ((size_t)(4 * rb + 2 * tb2) * 86 + (col0 >> 4) + 3 * chf) * 1024;
#pragma unroll
      for (int t = 0; t < 3; ++t) {
#pragma unroll
        for (int b = 0; b < 2; ++b) {
          float hv[8];
#pragma unroll
          for (int j = 0; j < 8; ++j) hv[j] = __builtin_fmaxf(acc[b][t][j], 0.f) * acc[b][t][8 + j];
          v4u o = {pkbf(hv[0], hv[1]), pkbf(hv[2], hv[3]), pkbf(hv[4], hv[5]), pkbf(hv[6], hv[7])};
          if (t > 0 || cb < 14) st16_sc1(o, (unsigned)(lane * 16), hb + (b * 86 + t) * 1024);
        }
      }
      sig_add(sm, S_STD, lane);
      STAMP(4);
    }
    const int th = w & 1, kh = w >> 1;
    int da[2];
    {
      int q = (lane >> 2) & 3, p = lane & 3;
#pragma unroll
      for (int e = 0; e < 2; ++e) da[e] = (4 * hh + q) * 128 + ((e ^ (q >> 1)) << 6) + (g4 & 1) * 32 + p * 8;
    }
    wait_ge(sm, S_FULL, 33);
    v4u wr[15][2];
#pragma unroll
    for (int i = 0; i < 15; ++i)
#pragma unroll
      for (int e = 0; e < 2; ++e) wr[i][e] = trfrag(sm, wd_slot(kh, i) + da[e], 1024);
    sig_add(sm, S_PAIR + kh * 4, lane);
    STAMP(5);
    WAITV(0);
    if (!csrole) {
      unsigned old = 0;
      if (lane == 0) old = __hip_atomic_fetch_add((LAS unsigned*)(sm + S_HCNT), 1u, __ATOMIC_RELAXED, __HIP_MEMORY_SCOPE_WORKGROUP);
      if (__builtin_amdgcn_readfirstlane(old) == 3 && lane == 0) st4_sc1(tag, (unsigned)(cb * 8), ws + WS_F3 + rb * 128);
    }
    STAMP(6);
    wait_ge(sm, S_PAIR + kh * 4, 2);
    {
      const unsigned dvo = (8 * th + (lane >> 3)) * 1024 + (((lane & 7) ^ ((g4 & 1) << 2)) << 4);
      const char* db = (const char*)wd + tn * 128 + (size_t)(43 * kh + 28) * 16384;
#pragma unroll
      for (int i = 0; i < 15; ++i) glds1(dvo, db + i * 16384, lb + wd_slot(kh, i) + th * 1024);
    }
    WAITV(0);
    sig_add(sm, S_REC, lane);
    if (w == 0) {
      unsigned fl;
#pragma unroll 1
      for (unsigned spins = 0; spins < SPIN_CAP; ++spins) {
        ld4_sc1(fl, (unsigned)(imin(l15, 14) * 8), ws + WS_F3 + (tm >> 1) * 128);
        asm volatile("s_waitcnt vmcnt(0)" : "+v"(fl) :: "memory");
        if (allv(fl == tag)) break;
        __builtin_amdgcn_s_sleep(POLL_SLEEP);
      }
      sig_set(sm, S_HRDY, 1);
    } else {
      wait_ge(sm, S_HRDY, 1);
    }
    STAMP(7);
    constexpr int HW = 14;
    v4u hf[HW];
    const char* hbase = ws + WS_H + ((size_t)(2 * tm + th) * 86 + 43 * kh) * 1024;
    const unsigned hl = lane * 16;
#pragma unroll
    for (int i = 0; i < HW; ++i) ld16_sc1(hf[i], hl, hbase + i * 1024);
    f32x16 oa[2];
#pragma unroll
    for (int e = 0; e < 2; ++e)
#pragma unroll
      for (int r = 0; r < 16; ++r) oa[e][r] = 0.f;
    v4u wq[43][2];
#pragma unroll
    for (int i = 0; i < 15; ++i) { wq[i][0] = wr[i][0]; wq[i][1] = wr[i][1]; }
#pragma unroll
    for (int i = 0; i < 43; ++i) {
      constexpr int PD = 3;
      const int n = i + PD, j = n < 28 ? n : n - 28;
      if (n == 18) wait_ge(sm, S_FULL, 34);
      if (n == 28) wait_ge(sm, S_REC, 4);
      asm volatile("s_waitcnt vmcnt(%1)" : "+v"(hf[i % HW]) : "i"(i + HW <= 43 ? HW - 1 : 42 - i) : "memory");
      SCHED();
      oa[0] = mfma32(wq[i][0], hf[i % HW], oa[0]);
      SCHED();
      if (n >= 15 && n < 43) wq[n][0] = trfrag(sm, wd_slot(kh, j) + da[0], 1024);
      if (n >= 15 && n < 43) wq[n][1] = trfrag(sm, wd_slot(kh, j) + da[1], 1024);
      SCHED();
      oa[1] = mfma32(wq[i][1], hf[i % HW], oa[1]);
      SCHED();
      if (i + HW < 43) ld16_sc1(hf[i % HW], hl, hbase + (i + HW) * 1024);
    }
    STAMP(8);
    wait_ge(sm, S_CONV, 4);
#pragma unroll
    for (int e = 0; e < 2; ++e)
#pragma unroll
      for (int i = 0; i < 4; ++i)
        *(LAS f32x4*)(sm + L_RED + kh * REDQ + (32 * th + l31) * REDP + (32 * e + 8 * i + 4 * hh) * 4) =
            f32x4{oa[e][4 * i], oa[e][4 * i + 1], oa[e][4 * i + 2], oa[e][4 * i + 3]};
    STAMP(9);
  } else {
    const unsigned dsk2 = ((const unsigned*)dsk)[cp];
    if (w < 6) {
      const int ch = w - 4, kd = 2 * cp + ch;
      unsigned la, ai, cr, ci;
      ld4(la, (unsigned)(lane * 4), lar + kd * 128); ld4(ai, (unsigned)(lane * 4), aim + kd * 128);
      ld4(cr, (unsigned)(lane * 4), cre + kd * 128); ld4(ci, (unsigned)(lane * 4), cim + kd * 128);
      const unsigned ldt2 = ((const unsigned*)ldt)[cp];
      BARRIER();
      const float dt = rbf(expacc(ch ? bfhi(ldt2) : bflo(ldt2)));
      asm volatile("s_waitcnt vmcnt(0)" : "+v"(la), "+v"(ai), "+v"(cr), "+v"(ci) :: "memory");
      STAMP(12);
      int mx = imax(imax((int)(cr & 0x7fff), (int)((cr >> 16) & 0x7fff)), imax((int)(ci & 0x7fff), (int)((ci >> 16) & 0x7fff)));
      const int ex = imin(imax(wave_maxi(mx) >> 7, 1), 253);
      const float sc = __builtin_bit_cast(float, (unsigned)(254 - ex) << 23);
      const float kscale = 2.0f * dt * __builtin_bit_cast(float, (unsigned)ex << 23);
      float ar2[2], ai2[2]; cf c0[2];
      ar2[0] = -rbf(expacc(bflo(la))) * dt * 1.44269504088896341f; ar2[1] = -rbf(expacc(bfhi(la))) * dt * 1.44269504088896341f;
      ai2[0] = bflo(ai) * dt * 0.15915494309189535f; ai2[1] = bfhi(ai) * dt * 0.15915494309189535f;
      c0[0].r = bflo(cr) * sc; c0[0].i = bflo(ci) * sc; c0[1].r = bfhi(cr) * sc; c0[1].i = bfhi(ci) * sc;
      const int kb = L_KC + ch * KCCH, kw = kb + lane * 8, kr = kb + l15 * KGP + g4 * 16;
      const int tb = L_TBL + ch * TBLCH;
      auto chain = [&](cf (&z)[2], const cf (&m)[2], int n, int row0, int rstep) NOPK __attribute__((always_inline)) {
#pragma unroll
        for (int i = 0; i < n; ++i) {
          ST64(kw + (row0 + i * rstep) * KGP, (v2u{pkh(z[0].r, z[0].i), pkh(z[1].r, z[1].i)}));
          z[0] = cmul(z[0], m[0]); z[1] = cmul(z[1], m[1]);
        }
      };
      cf m[2], m128[2], z[2], g[2];
      v4u yf[8];
      m[0] = cexpp(ar2[0], -ai2[0], 8.f); m[1] = cexpp(ar2[1], -ai2[1], 8.f);
      z[0].r = 1.f; z[0].i = 0.f; z[1] = z[0];
      chain(z, m, 16, 0, 1);
#pragma unroll
      for (int s = 0; s < 8; ++s) yf[s] = LD128(kr + s * 64);
      MEMF();
      v4u xf[3][8];
#define KG_GRAB(t) do { MEMF(); _Pragma("unroll") for (int s = 0; s < 8; ++s) xf[t][s] = LD128(kr + s * 64); MEMF(); } while (0)
      m128[0] = cexpp(ar2[0], ai2[0], 128.f); m128[1] = cexpp(ar2[1], ai2[1], 128.f);
      m[0] = cexpp(ar2[0], ai2[0], 1.f); m[1] = cexpp(ar2[1], ai2[1], 1.f);
      z[0] = c0[0]; z[1] = c0[1];
      chain(z, m, 8, 0, 1);
      g[0] = cmul(c0[0], m128[0]); g[1] = cmul(c0[1], m128[1]); z[0] = g[0]; z[1] = g[1];
      chain(z, m, 8, 8, 1);
      KG_GRAB(0);
      m[0] = cmul(m[0], m[0]); m[1] = cmul(m[1], m[1]);
      g[0] = cmul(g[0], m128[0]); g[1] = cmul(g[1], m128[1]); z[0] = g[0]; z[1] = g[1];
      chain(z, m, 4, 0, 1);
      g[0] = cmul(g[0], m128[0]); g[1] = cmul(g[1], m128[1]); z[0] = g[0]; z[1] = g[1];
      chain(z, m, 4, 4, 1);
      m[0] = cmul(m[0], m[0]); m[1] = cmul(m[1], m[1]);
      g[0] = cmul(g[0], m128[0]); g[1] = cmul(g[1], m128[1]);
      z[0] = cmul(g[0], m[0]); z[1] = cmul(g[1], m[1]);
      chain(z, m128, 4, 9, 2);
      chain(g, m128, 4, 8, 2);
      KG_GRAB(1);
      chain(g, m128, 9, 0, 1);
      KG_GRAB(2);
      STAMP(22);
      {
        f32x4 a[3];
#pragma unroll
        for (int t = 0; t < 3; ++t) a[t] = f32x4{0.f, 0.f, 0.f, 0.f};
#pragma unroll
        for (int s = 0; s < 8; ++s)
#pragma unroll
          for (int t = 0; t < 3; ++t) a[t] = mfma16h(xf[t][s], yf[s], a[t]);
        ST64(tb + 2 * (128 * (g4 >> 1) + 8 * l15 + 4 * (g4 & 1)), (v2u{pkbf(a[0][0] * kscale, a[0][1] * kscale), pkbf(a[0][2] * kscale, a[0][3] * kscale)}));
        const int i0 = g4 < 2 ? 256 + 64 * g4 + 4 * l15 : 384 + 64 * (g4 - 2) + 2 * l15, i2 = g4 < 2 ? i0 + 2 : i0 + 32;
        *(LAS unsigned*)(sm + tb + 2 * i0) = pkbf(a[1][0] * kscale, a[1][1] * kscale);
        *(LAS unsigned*)(sm + tb + 2 * i2) = pkbf(a[1][2] * kscale, a[1][3] * kscale);
        const int i3 = tb + 2 * (512 + 64 * g4 + l15);
        const unsigned p01 = pkbf(a[2][0] * kscale, a[2][1] * kscale), p23 = pkbf(a[2][2] * kscale, a[2][3] * kscale);
        ST16(i3, p01); ST16(i3 + 32, p01 >> 16); ST16(i3 + 64, p23); ST16(i3 + 96, p23 >> 16);
        MEMF();
      }
      STAMP(13);
      wait_ge(sm, S_LN, 1);
      {
        v4u d[4];
        const int nl = 63 - lane;
#pragma unroll
        for (int r = 0; r < 2; ++r) {
          const int a = tb + 2 * (512 + 64 * (1 - r) + nl);
          unsigned lo = LD16(a), hi = LD16(a + 2);
          unsigned c = (nl & 1) ? hi : lo;
          d[r] = v4u{hi | (hi << 16), hi | (c << 16), lo | (lo << 16), lo | (lo << 16)};
        }
        {
          const int a = tb + 2 * (384 + 2 * nl);
          unsigned w0 = *(LAS const unsigned*)(sm + a), p = LD16(a + 4);
          d[2] = v4u{p | (p << 16), __builtin_amdgcn_perm(w0, w0, 0x03020302u), __builtin_amdgcn_perm(w0, w0, 0x01000302u), __builtin_amdgcn_perm(w0, w0, 0x01000100u)};
        }
        {
          const bool s1 = lane < 32;
          const int a = tb + (s1 ? 2 * (256 + 4 * (nl - 32)) : 16 * nl);
          v2u wl = *(LAS const v2u*)(sm + a), wh = *(LAS const v2u*)(sm + a + 8);
          d[3].x = s1 ? __builtin_amdgcn_perm(wl.y, wh.x, 0x07060100u) : __builtin_amdgcn_perm(wh.y, wh.y, 0x01000302u);
          d[3].y = s1 ? __builtin_amdgcn_perm(wl.y, wl.y, 0x01000100u) : __builtin_amdgcn_perm(wh.x, wh.x, 0x01000302u);
          d[3].z = s1 ? __builtin_amdgcn_perm(wl.x, wl.y, 0x07060100u) : __builtin_amdgcn_perm(wl.y, wl.y, 0x01000302u);
          d[3].w = __builtin_amdgcn_perm(wl.x, wl.x, s1 ? 0x01000100u : 0x01000302u);
        }
        const int cbs = kb + lane * 16;
#pragma unroll
        for (int r = 0; r < 4; ++r) ST128(cbs + r * 1024, d[r]);
        if (lane < 8) ST128(kb + (lane >> 1) * KCP + 4096 + (lane & 1) * 16, (v4u{0u, 0u, 0u, 0u}));
        MEMF();
#pragma unroll
        for (int r = 0; r < 4; ++r) {
          v2u nx = *(LAS const v2u*)(sm + cbs + r * 1024 + 16);
          unsigned o0 = __builtin_amdgcn_alignbit(d[r].y, d[r].x, 16), o1 = __builtin_amdgcn_alignbit(d[r].z, d[r].y, 16),
                   o2 = __builtin_amdgcn_alignbit(d[r].w, d[r].z, 16), o3 = __builtin_amdgcn_alignbit(nx.x, d[r].w, 16),
                   o4 = __builtin_amdgcn_alignbit(nx.y, nx.x, 16);
          ST128(cbs + r * 1024 + KCP, (v4u{o0, o1, o2, o3}));
          ST128(cbs + r * 1024 + 2 * KCP, (v4u{d[r].y, d[r].z, d[r].w, nx.x}));
          ST128(cbs + r * 1024 + 3 * KCP, (v4u{o1, o2, o3, o4}));
        }
      }
      sig_add(sm, S_KRDY, lane);
      STAMP(14);
    } else if (w == 6) {
      v4u xv[8];
      unsigned ev; ld4_sc1(ev, 0u, ws + WS_E);
      const int r = lane >> 3, c = lane & 7;
#pragma unroll
      for (int i = 0; i < 8; ++i) ld16(xv[i], (unsigned)(r * 1024 + c * 16 + i * 128), x + (size_t)bid * 4096);
      BARRIER();
      asm volatile("s_waitcnt vmcnt(0)" : "+v"(ev), "+v"(xv[0]), "+v"(xv[1]), "+v"(xv[2]), "+v"(xv[3]), "+v"(xv[4]), "+v"(xv[5]), "+v"(xv[6]), "+v"(xv[7]) :: "memory");
      tag = __builtin_amdgcn_readfirstlane(ev) + 0x9E3779B9u;
      STAMP(18);
      float v[64];
      float s0 = 0.f, s1 = 0.f, s2 = 0.f, s3 = 0.f;
#pragma unroll
      for (int i = 0; i < 8; ++i) {
#pragma unroll
        for (int j = 0; j < 4; ++j) { v[8 * i + 2 * j] = bflo(xv[i][j]); v[8 * i + 2 * j + 1] = bfhi(xv[i][j]); }
        s0 += v[8 * i] + v[8 * i + 4]; s1 += v[8 * i + 1] + v[8 * i + 5]; s2 += v[8 * i + 2] + v[8 * i + 6]; s3 += v[8 * i + 3] + v[8 * i + 7];
      }
      const float mean = sum8((s0 + s1) + (s2 + s3)) * (1.0f / 512);
      const float m = rbf(mean);
      s0 = s1 = s2 = s3 = 0.f;
#pragma unroll
      for (int i = 0; i < 64; i += 4) {
        v[i] -= m; v[i + 1] -= m; v[i + 2] -= m; v[i + 3] -= m;
        s0 += v[i] * v[i]; s1 += v[i + 1] * v[i + 1]; s2 += v[i + 2] * v[i + 2]; s3 += v[i + 3] * v[i + 3];
      }
      const float dm = mean - m;
      const float var = rbf(__builtin_fmaxf(sum8((s0 + s1) + (s2 + s3)) * (1.0f / 512) - dm * dm, 0.f));
      const float sd = rbf(__builtin_amdgcn_sqrtf(rbf(var + 1e-5f)));
      const float rs = __builtin_amdgcn_rcpf(sd);
      const int lw = L_KC + (r >> 2) * KCCH + LNOFF + (r & 3) * LNP + c * 16;
#pragma unroll
      for (int i = 0; i < 8; ++i) {
        v4u o;
#pragma unroll
        for (int j = 0; j < 4; ++j) { unsigned d = pkbf(v[8 * i + 2 * j], v[8 * i + 2 * j + 1]); o[j] = pkbf(bflo(d) * rs, bfhi(d) * rs); }
        ST128(lw + i * 128, o);
      }
      MEMF();
      {
        v2u sg = {pkbf(-m, sd), tag};
        if (c == 0) st8_sc1(sg, (unsigned)(r * 8), ws + WS_STAT + bid * 64);
        const int q = (lane >> 2) & 3, p = lane & 3;
        const int ta = L_KC + (p & 1) * KCCH + LNOFF + q * LNP + (2 * g4 + (p >> 1)) * 8;
#pragma unroll
        for (int i = 0; i < 16; ++i) {
          v2u t = LDTR(ta + i * 64);
          st16_sc1(v4u{t.x, tag, t.y, tag}, (unsigned)(lane * 16), ws + WS_XG + (size_t)bid * 16384 + i * 1024);
        }
      }
      sig_set(sm, S_TAG, tag);
      sig_set(sm, S_LN, 1);
      STAMP(19);
      wait_free(sm, 5);
      v4u ug[16], sg2;
      const unsigned ugo = (unsigned)((lane >> 2) * 16384 + ((lane >> 1) & 1) * 64 + (lane & 1) * 16);
      const char* ugbase = ws + WS_XG + (cp >> 1) * 128 + (cp & 1) * 32;
#pragma unroll 1
      for (unsigned spins = 0; spins < SPIN_CAP; ++spins) {
#pragma unroll
        for (int i = 0; i < 16; ++i) ld16_sc1(ug[i], ugo, ugbase + i * (16 * 16384));
        ld16_sc1(sg2, (unsigned)(lane * 16), ws + WS_STAT + rb * 1024);
        asm volatile("s_waitcnt vmcnt(0)" : "+v"(ug[0]), "+v"(ug[1]), "+v"(ug[2]), "+v"(ug[3]), "+v"(ug[4]), "+v"(ug[5]), "+v"(ug[6]), "+v"(ug[7]),
                     "+v"(ug[8]), "+v"(ug[9]), "+v"(ug[10]), "+v"(ug[11]), "+v"(ug[12]), "+v"(ug[13]), "+v"(ug[14]), "+v"(ug[15]), "+v"(sg2) :: "memory");
        unsigned bad = (sg2.y ^ tag) | (sg2.w ^ tag);
#pragma unroll
        for (int i = 0; i < 16; ++i) bad |= (ug[i].y ^ tag) | (ug[i].w ^ tag);
        if (allv(bad == 0)) break;
        __builtin_amdgcn_s_sleep(8);
      }
      STAMP(20);
      if (bid == 0 && lane == 0) st4_sc1(tag, 0u, ws + WS_E);
      *(LAS unsigned*)(sm + L_NM + lane * 4) = (sg2.x & 0xffffu) | (sg2.z << 16);
      wait_free(sm, 6);
      const int ua = L_U + (lane & 1) * UCH + 480 + (lane >> 2) * 16 + ((lane >> 1) & 1) * 8;
#pragma unroll
      for (int i = 0; i < 16; ++i) ST64(ua + i * 256, (v2u{ug[i].x, ug[i].z}));
      if (lane < 60) ST128(L_U + (lane / 30) * UCH + (lane % 30) * 16, (v4u{0u, 0u, 0u, 0u}));
      sig_set(sm, S_URDY, 1);
      STAMP(21);
      if (!csrole) {
        v2u cg[3];
#pragma unroll 1
        for (unsigned spins = 0; spins < SPIN_CAP; ++spins) {
#pragma unroll
          for (int i = 0; i < 3; ++i) ld8_sc1(cg[i], (unsigned)(lane * 8), ws + WS_CS + cb * 2048 + i * 512);
          asm volatile("s_waitcnt vmcnt(0)" : "+v"(cg[0]), "+v"(cg[1]), "+v"(cg[2]) :: "memory");
          if (allv(((cg[0].y ^ tag) | (cg[1].y ^ tag) | (cg[2].y ^ tag)) == 0)) break;
          __builtin_amdgcn_s_sleep(4);
        }
#pragma unroll
        for (int i = 0; i < 3; ++i) {
          float c = __builtin_bit_cast(float, cg[i].x);
          float c0 = rbf(c), c1 = rbf(c - c0), c2 = (c - c0) - c1;
          ST64(L_CSX + (64 * i + lane) * 8, (v2u{pkbf(c0, c1), pkbf(c2, 0.f)}));
        }
        sig_set(sm, S_CS, 1);
        STAMP(23);
      }
    }
    wait_ge(sm, S_URDY, 1);
    wait_ge(sm, S_KRDY, 2);
    tag = lds_rd(sm, S_TAG);
    if (w == 4) STAMP(15);
    const int cch = w & 1, ctg = (w >> 1) & 1;
    f32x4 ca[4];
#pragma unroll
    for (int i = 0; i < 4; ++i) ca[i] = f32x4{0.f, 0.f, 0.f, 0.f};
    {
      int ka = L_KC + cch * KCCH + ((~l15) & 3) * KCP + 2 * (2028 + 8 * g4 - 4 * (l15 >> 2)) - 7 * 512;
      int ub = L_U + cch * UCH + 480 + 32 * (l15 - 1) + 16 * g4;
#define CV_LOAD(A, B, n) do { _Pragma("unroll") for (int q = 0; q < n; ++q) { \
          v2u p0 = LD64V(ka + (7 - q) * 512), p1 = LD64V(ka + (7 - q) * 512 + 8); \
          A[q] = v4u{p0.x, p0.y, p1.x, p1.y}; B[q] = LD128(ub + q * 512); } ka -= 64; ub -= 64; } while (0)
#define CV_MMA0(A, B) do { ca[0] = mfma16(A[0], B[0], ca[0]); ca[1] = mfma16(A[0], B[1], ca[1]); ca[1] = mfma16(A[1], B[0], ca[1]); \
          _Pragma("unroll") for (int q = 0; q < 7; ++q) { ca[2] = mfma16(A[q], B[6 - q], ca[2]); ca[3] = mfma16(A[q], B[7 - q], ca[3]); } \
          ca[3] = mfma16(A[7], B[0], ca[3]); } while (0)
#define CV_MMA1(A, B) do { _Pragma("unroll") for (int t = 2; t < 6; ++t) _Pragma("unroll") for (int q = 0; q <= t; ++q) \
          ca[t - 2] = mfma16(A[q], B[t - q], ca[t - 2]); } while (0)
#define CV_IL(n, k) do { _Pragma("unroll") for (int z = 0; z < n; ++z) { __builtin_amdgcn_sched_group_barrier(0x8, 1, 0); \
          __builtin_amdgcn_sched_group_barrier(0x100, k, 0); } __builtin_amdgcn_sched_group_barrier(0x8, 18 - n, 0); } while (0)
      if (ctg == 0) {
        v4u A0[8], B0[8], A1[8], B1[8];
        CV_LOAD(A0, B0, 8);
#pragma unroll 1
        for (int it = 0; it < 4; ++it) {
          SCHED(); CV_LOAD(A1, B1, 8); CV_MMA0(A0, B0); CV_IL(12, 2); SCHED();
          { const int bk = it == 3 ? 64 : 0; ka += bk; ub += bk; }
          SCHED(); CV_LOAD(A0, B0, 8); CV_MMA0(A1, B1); CV_IL(12, 2); SCHED();
        }
      } else {
        v4u A0[6], B0[6], A1[6], B1[6];
        CV_LOAD(A0, B0, 6);
#pragma unroll 1
        for (int it = 0; it < 4; ++it) {
          SCHED(); CV_LOAD(A1, B1, 6); CV_MMA1(A0, B0); CV_IL(9, 2); SCHED();
          { const int bk = it == 3 ? 64 : 0; ka += bk; ub += bk; }
          SCHED(); CV_LOAD(A0, B0, 6); CV_MMA1(A1, B1); CV_IL(9, 2); SCHED();
        }
      }
    }
    {
      const float dk = cch ? bfhi(dsk2) : bflo(dsk2);
      const int lo = 16 * l15 + 4 * g4;
      char* yb = ws + WS_YG + (size_t)(2 * cp + cch) * 8192;
#pragma unroll
      for (int i = 0; i < 4; ++i) {
        const int t = ctg ? 2 + i : (i < 2 ? i : 4 + i);
        v2u u = *(LAS const v2u*)(sm + L_U + cch * UCH + 480 + (256 * t + lo) * 2);
        f32x4 y = ca[i];
        unsigned y01 = pkbf(y.x, y.y), y23 = pkbf(y.z, y.w);
        unsigned s01 = pkbf(bflo(u.x) * dk, bfhi(u.x) * dk), s23 = pkbf(bflo(u.y) * dk, bfhi(u.y) * dk);
        v4u o = {pkbf(bflo(y01) + bflo(s01), bfhi(y01) + bfhi(s01)), tag, pkbf(bflo(y23) + bflo(s23), bfhi(y23) + bfhi(s23)), tag};
        st16_sc1(o, (unsigned)(lo * 4), yb + t * 1024);
      }
    }
    sig_add(sm, S_CONV, lane);
    if (w == 4) STAMP(16);
    {
      const int xw = w - 4;
      v4u xr[2], yg[4]; v2u sg;
      const unsigned xo = (unsigned)((8 * xw + (lane >> 3)) * 1024 + (lane & 7) * 16);
      const char* xb = (const char*)x + (size_t)tm * 65536 + tn * 128;
      ld16(xr[0], xo, xb); ld16(xr[1], xo, xb + 32768);
      const unsigned ygo = (unsigned)((16 * xw + g4) * 8192 + l15 * 16);
      const char* ygbase = ws + WS_YG + (size_t)tn * 64 * 8192 + tm * 256;
#pragma unroll 1
      for (unsigned spins = 0; spins < SPIN_CAP; ++spins) {
#pragma unroll
        for (int i = 0; i < 4; ++i) ld16_sc1(yg[i], ygo, ygbase + i * 4 * 8192);
        ld8_sc1(sg, (unsigned)(lane * 8), ws + WS_STAT + tm * 512);
        asm volatile("s_waitcnt vmcnt(0)" : "+v"(yg[0]), "+v"(yg[1]), "+v"(yg[2]), "+v"(yg[3]), "+v"(sg), "+v"(xr[0]), "+v"(xr[1]) :: "memory");
        unsigned bad = sg.y ^ tag;
#pragma unroll
        for (int i = 0; i < 4; ++i) bad |= (yg[i].y ^ tag) | (yg[i].w ^ tag);
        if (allv(bad == 0)) break;
        __builtin_amdgcn_s_sleep(8);
      }
      wait_ge(sm, S_CONV, 4);
#pragma unroll
      for (int i = 0; i < 4; ++i) {
        const int ab = L_AIMG + (4 * l15) * 128 + (((2 * xw + (i >> 1)) ^ (l15 & 7)) << 4) + ((i & 1) * 4 + g4) * 2;
        ST16(ab, yg[i].x); ST16(ab + 128, yg[i].x >> 16); ST16(ab + 256, yg[i].z); ST16(ab + 384, yg[i].z >> 16);
      }
      if (xw == 0) { float rs = __builtin_amdgcn_rcpf(bfhi(sg.x)); *(LAS float*)(sm + L_RS2 + lane * 4) = rs * rs; }
      sig_add(sm, S_AIMG, lane);
      wait_ge(sm, S_AIMG, 4);
#pragma unroll
      for (int i = 0; i < 2; ++i) {
        const int tok = 32 * i + 8 * xw + (lane >> 3);
        const int a = L_AIMG + tok * 128 + (((lane & 7) ^ ((tok >> 2) & 7)) << 4);
        v4u av = LD128(a), o;
#pragma unroll
        for (int j = 0; j < 4; ++j) o[j] = pkbf(bflo(xr[i][j]) + bflo(av[j]), bfhi(xr[i][j]) + bfhi(av[j]));
        ST128(a, o);
      }
      if (w == 4) STAMP(17);
    }
  }
  BARRIER();
  if (w == 0) STAMP(10);
  {
    const int tok = tid >> 3, c = tid & 7;
    const LAS f32x4* rp = (const LAS f32x4*)(sm + L_RED + tok * REDP + c * 32);
    f32x4 s0 = rp[0] + rp[REDQ / 16], s1 = rp[1] + rp[REDQ / 16 + 1];
    const float rs2 = *(LAS const float*)(sm + L_RS2 + tok * 4);
    v4u rv = LD128(L_AIMG + tok * 128 + ((c ^ ((tok >> 2) & 7)) << 4));
    unsigned g0 = pkbf(s0.x * rs2, s0.y * rs2), g1 = pkbf(s0.z * rs2, s0.w * rs2), g2 = pkbf(s1.x * rs2, s1.y * rs2), g3 = pkbf(s1.z * rs2, s1.w * rs2);
    v4u o = {pkbf(bflo(rv.x) + bflo(g0), bfhi(rv.x) + bfhi(g0)), pkbf(bflo(rv.y) + bflo(g1), bfhi(rv.y) + bfhi(g1)),
             pkbf(bflo(rv.z) + bflo(g2), bfhi(rv.z) + bfhi(g2)), pkbf(bflo(rv.w) + bflo(g3), bfhi(rv.w) + bfhi(g3))};
    st16(o, oo, (char*)out + (size_t)tm * 65536 + tn * 128);
  }
}

extern "C" void kernel_launch(void* const* d_in, const int* in_sizes, int n_in,
                              void* d_out, int out_size, void* d_ws, size_t ws_size,
                              hipStream_t stream) {
  fused_kernel<<<256, 512, 0, stream>>>(
      (const u16*)d_in[0], (const u16*)d_in[1], (const u16*)d_in[2], (const u16*)d_in[3], (const u16*)d_in[4],
      (const u16*)d_in[5], (const u16*)d_in[6], (const u16*)d_in[8], (const u16*)d_in[9], (const u16*)d_in[10],
      (u16*)d_out, (char*)d_ws);
}
```

```cpp
#define CODE_BYTES 29440
#define WARM_TLB 1
#define WARM_CODE 0
#define POLL_SLEEP 4
#define ROT 0
#include <hip/hip_runtime.h>
#include <hip/hip_bf16.h>
#include <stdint.h>

typedef unsigned short u16;
typedef unsigned v4u __attribute__((ext_vector_type(4)));
typedef unsigned v2u __attribute__((ext_vector_type(2)));
typedef short v4s __attribute__((ext_vector_type(4)));
typedef float f32x2 __attribute__((ext_vector_type(2)));
typedef float f32x4 __attribute__((ext_vector_type(4)));
typedef float f32x16 __attribute__((ext_vector_type(16)));
typedef __bf16 bf16x2 __attribute__((ext_vector_type(2)));
typedef __bf16 bf16x8 __attribute__((ext_vector_type(8)));
typedef _Float16 f16x2 __attribute__((ext_vector_type(2)));
typedef _Float16 f16x8 __attribute__((ext_vector_type(8)));
#define LAS __attribute__((address_space(3)))
#if defined(__HIP_DEVICE_COMPILE__)
#define NOPK __attribute__((target("no-packed-fp32-ops")))
#else
#define NOPK
#endif
#define DEVI NOPK __device__ __forceinline__

constexpr size_t WS_E = 0;
constexpr size_t WS_F3 = 4096;
constexpr size_t WS_STAT = 8192;
constexpr size_t WS_CS = 32768;
constexpr size_t WS_XG = 65536;
constexpr size_t WS_YG = WS_XG + (4u << 20);
constexpr size_t WS_H = WS_YG + (4u << 20);

constexpr int SLOT = 40960;
constexpr int L_U = 114688, UCH = 4576;
constexpr int L_KC = 123904, KCP = 4160, KCCH = 4 * KCP;
constexpr int KGP = 528, LNP = 1088, LNOFF = 8448;
constexpr int L_TBL = 157184, TBLCH = 1568;
constexpr int L_CSX = 160320;
constexpr int L_R2 = 81920, L_R0 = 0, L_R1 = 40960;
constexpr int L_RED = 114688, REDP = 272, REDQ = 64 * REDP;
constexpr int L_AIMG = 149504, L_RS2 = 157696;
constexpr int L_NM = 163392;
constexpr int L_SYNC = 163712;
constexpr int S_FULL = L_SYNC, S_LN = L_SYNC + 4, S_KRDY = L_SYNC + 8, S_URDY = L_SYNC + 12, S_FREE = L_SYNC + 16,
              S_HCNT = L_SYNC + 32, S_PRE = L_SYNC + 44, S_CONV = L_SYNC + 48, S_AIMG = L_SYNC + 52, S_TAG = L_SYNC + 56, S_CS = L_SYNC + 60, S_STD = L_SYNC + 36, S_HRDY = L_SYNC + 40, S_PAIR = L_SYNC + 64, S_REC = L_SYNC + 72;
constexpr int LDS_BYTES = 163840;

constexpr unsigned SPIN_CAP = 1u << 16, LSPIN_CAP = 1u << 22;

#define BARRIER() asm volatile("s_waitcnt lgkmcnt(0)\n\ts_barrier" ::: "memory")
#define WAITV(n) asm volatile("s_waitcnt vmcnt(" #n ")" ::: "memory")
#define SCHED() __builtin_amdgcn_sched_barrier(0)
#define MEMF() asm volatile("" ::: "memory")

DEVI void glds4(unsigned v0, unsigned v1, unsigned v2, unsigned v3, const void* sb, unsigned lds) {
  asm volatile("s_mov_b32 m0, %5\n\ts_nop 0\n\tglobal_load_lds_dwordx4 %0, %4\n\tglobal_load_lds_dwordx4 %1, %4 offset:1024\n\t"
               "global_load_lds_dwordx4 %2, %4 offset:2048\n\tglobal_load_lds_dwordx4 %3, %4 offset:3072"
               :: "v"(v0), "v"(v1), "v"(v2), "v"(v3), "s"(sb), "s"(lds) : "memory");
}
DEVI void glds3(unsigned v0, unsigned v1, unsigned v2, const void* sb, unsigned lds) {
  asm volatile("s_mov_b32 m0, %4\n\ts_nop 0\n\tglobal_load_lds_dwordx4 %0, %3\n\tglobal_load_lds_dwordx4 %1, %3 offset:1024\n\t"
               "global_load_lds_dwordx4 %2, %3 offset:2048"
               :: "v"(v0), "v"(v1), "v"(v2), "s"(sb), "s"(lds) : "memory");
}
DEVI void glds1(unsigned v0, const void* sb, unsigned lds) {
  asm volatile("s_mov_b32 m0, %2\n\ts_nop 0\n\tglobal_load_lds_dwordx4 %0, %1" :: "v"(v0), "s"(sb), "s"(lds) : "memory");
}
DEVI void glds2(unsigned v0, unsigned v1, const void* sb, unsigned lds) {
  asm volatile("s_mov_b32 m0, %3\n\ts_nop 0\n\tglobal_load_lds_dwordx4 %0, %2\n\tglobal_load_lds_dwordx4 %1, %2 offset:1024"
               :: "v"(v0), "v"(v1), "s"(sb), "s"(lds) : "memory");
}
DEVI void ld16_sc1(v4u& d, unsigned voff, const void* sbase) {
  asm volatile("global_load_dwordx4 %0, %1, %2 sc1" : "=v"(d) : "v"(voff), "s"(sbase) : "memory");
}
DEVI void ld16(v4u& d, unsigned voff, const void* sbase) {
  asm volatile("global_load_dwordx4 %0, %1, %2" : "=v"(d) : "v"(voff), "s"(sbase) : "memory");
}
DEVI void ld8_sc1(v2u& d, unsigned voff, const void* sbase) {
  asm volatile("global_load_dwordx2 %0, %1, %2 sc1" : "=v"(d) : "v"(voff), "s"(sbase) : "memory");
}
DEVI void ld4_sc1(unsigned& d, unsigned voff, const void* sbase) {
  asm volatile("global_load_dword %0, %1, %2 sc1" : "=v"(d) : "v"(voff), "s"(sbase) : "memory");
}
DEVI void ld4(unsigned& d, unsigned voff, const void* sbase) {
  asm volatile("global_load_dword %0, %1, %2" : "=v"(d) : "v"(voff), "s"(sbase) : "memory");
}
DEVI void st16_sc1(v4u d, unsigned voff, void* sbase) {
  asm volatile("global_store_dwordx4 %0, %1, %2 sc1\n\ts_nop 1" :: "v"(voff), "v"(d), "s"(sbase) : "memory");
}
DEVI void st16(v4u d, unsigned voff, void* sbase) {
  asm volatile("global_store_dwordx4 %0, %1, %2\n\ts_nop 1" :: "v"(voff), "v"(d), "s"(sbase) : "memory");
}
DEVI void st8_sc1(v2u d, unsigned voff, void* sbase) {
  asm volatile("global_store_dwordx2 %0, %1, %2 sc1" :: "v"(voff), "v"(d), "s"(sbase) : "memory");
}
DEVI void st4_sc1(unsigned d, unsigned voff, void* sbase) {
  asm volatile("global_store_dword %0, %1, %2 sc1" :: "v"(voff), "v"(d), "s"(sbase) : "memory");
}
#define PIN(x) asm volatile("" : "+v"(x))

DEVI float bflo(unsigned w) { return __builtin_bit_cast(float, w << 16); }
DEVI float bfhi(unsigned w) { return __builtin_bit_cast(float, w & 0xffff0000u); }
DEVI unsigned pkbf(float a, float b) { f32x2 f = {a, b}; return __builtin_bit_cast(unsigned, __builtin_convertvector(f, bf16x2)); }
DEVI unsigned pkh(float a, float b) { f32x2 f = {a, b}; return __builtin_bit_cast(unsigned, __builtin_convertvector(f, f16x2)); }
DEVI float rbf(float a) { return bflo(pkbf(a, a)); }
DEVI int imax(int a, int b) { return a > b ? a : b; }
DEVI int imin(int a, int b) { return a < b ? a : b; }
template <int CTRL> DEVI float dppf(float v) {
  return __builtin_bit_cast(float, __builtin_amdgcn_update_dpp(0, __builtin_bit_cast(int, v), CTRL, 0xf, 0xf, true));
}
DEVI float sum8(float v) { v += dppf<0xB1>(v); v += dppf<0x4E>(v); v += dppf<0x141>(v); return v; }
DEVI int wave_maxi(int v) {
  v = imax(v, __builtin_amdgcn_update_dpp(0, v, 0x111, 0xf, 0xf, true));
  v = imax(v, __builtin_amdgcn_update_dpp(0, v, 0x112, 0xf, 0xf, true));
  v = imax(v, __builtin_amdgcn_update_dpp(0, v, 0x114, 0xf, 0xf, true));
  v = imax(v, __builtin_amdgcn_update_dpp(0, v, 0x118, 0xf, 0xf, true));
  v = imax(v, __builtin_amdgcn_update_dpp(0, v, 0x142, 0xa, 0xf, true));
  v = imax(v, __builtin_amdgcn_update_dpp(0, v, 0x143, 0xc, 0xf, true));
  return __builtin_amdgcn_readlane(v, 63);
}
DEVI float expacc(float x) {
  const float L = 1.44269504088896341f, Ll = 1.925963033500011e-8f;
  float hi = x * L, lo = __builtin_fmaf(x, L, -hi) + x * Ll;
  float e = __builtin_amdgcn_exp2f(hi);
  return __builtin_fmaf(e, lo * 0.6931471805599453f, e);
}
struct cf { float r, i; };
DEVI cf cmul(cf a, cf b) { cf c; c.r = a.r * b.r - a.i * b.i; c.i = a.r * b.i + a.i * b.r; return c; }
DEVI cf cexpp(float ar2, float ai2, float p) {
  float e = __builtin_amdgcn_exp2f(ar2 * p);
  float r = __builtin_amdgcn_fractf(ai2 * p);
  cf c; c.r = e * __builtin_amdgcn_cosf(r); c.i = e * __builtin_amdgcn_sinf(r); return c;
}
DEVI f32x16 mfma32(v4u a, v4u b, f32x16 c) {
  return __builtin_amdgcn_mfma_f32_32x32x16_bf16(__builtin_bit_cast(bf16x8, a), __builtin_bit_cast(bf16x8, b), c, 0, 0, 0);
}
DEVI f32x4 mfma16(v4u a, v4u b, f32x4 c) {
  return __builtin_amdgcn_mfma_f32_16x16x32_bf16(__builtin_bit_cast(bf16x8, a), __builtin_bit_cast(bf16x8, b), c, 0, 0, 0);
}
DEVI f32x4 mfma16h(v4u a, v4u b, f32x4 c) {
  return __builtin_amdgcn_mfma_f32_16x16x32_f16(__builtin_bit_cast(f16x8, a), __builtin_bit_cast(f16x8, b), c, 0, 0, 0);
}
DEVI float dot2ones(unsigned w, float acc) {
  bf16x2 ones = {(__bf16)1.0f, (__bf16)1.0f};
  return __builtin_amdgcn_fdot2_f32_bf16(__builtin_bit_cast(bf16x2, w), ones, acc, false);
}
DEVI bool allv(bool p) { return __builtin_amdgcn_ballot_w64(p) == ~0ull; }

#define STAMP(i)

#define LD128(a) (*(LAS const v4u*)(sm + (a)))
#define LD64V(a) (*(LAS const volatile v2u*)(sm + (a)))
#define LDTR(a) __builtin_bit_cast(v2u, __builtin_amdgcn_ds_read_tr16_b64_v4i16((LAS v4s*)(sm + (a))))
#define ST128(a, v) (*(LAS v4u*)(sm + (a)) = (v))
#define ST64(a, v) (*(LAS v2u*)(sm + (a)) = (v))
#define ST16(a, v) (*(LAS u16*)(sm + (a)) = (u16)(v))
#define LD16(a) ((unsigned)*(LAS const u16*)(sm + (a)))
DEVI v4u trfrag(LAS char* sm, int a, int d) { v2u p = LDTR(a), q = LDTR(a + d); return v4u{p.x, p.y, q.x, q.y}; }
DEVI unsigned lds_rd(LAS char* sm, int a) { return __builtin_amdgcn_readfirstlane(*(LAS volatile unsigned*)(sm + a)); }
DEVI unsigned spin_ge(unsigned vaddr, unsigned n) {
  unsigned cnt, tmp, cur;
  asm volatile("s_mov_b32 %1, 0x400000\n"
               "1:\n\tds_read_b32 %2, %3\n\ts_waitcnt lgkmcnt(0)\n\tv_readfirstlane_b32 %0, %2\n\ts_sub_u32 %1, %1, 1\n\t"
               "s_cbranch_scc1 2f\n\ts_cmp_lt_u32 %0, %4\n\ts_cbranch_scc1 1b\n2:"
               : "=&s"(cur), "=&s"(cnt), "=&v"(tmp) : "v"(vaddr), "s"(__builtin_amdgcn_readfirstlane(n)) : "memory", "scc");
  return __builtin_amdgcn_readfirstlane(cur);
}
DEVI void wait_ge(LAS char* sm, int a, unsigned n) {
  unsigned cnt, tmp, cur;
  asm volatile("s_mov_b32 %1, 0x400000\n"
               "1:\n\tds_read_b32 %2, %3\n\ts_waitcnt lgkmcnt(0)\n\tv_readfirstlane_b32 %0, %2\n\ts_cmp_ge_u32 %0, %4\n\t"
               "s_cbranch_scc1 2f\n\ts_sleep 1\n\ts_sub_u32 %1, %1, 1\n\ts_cbranch_scc0 1b\n2:"
               : "=&s"(cur), "=&s"(cnt), "=&v"(tmp) : "v"((unsigned)(uintptr_t)(sm + a)), "s"(n) : "memory", "scc");
}
DEVI f32x4 lanesum4(unsigned a, unsigned b, f32x4 c) {
  const v4s ones = {0x3f80, 0x3f80, 0x3f80, 0x3f80};
  return __builtin_amdgcn_mfma_f32_4x4x4bf16_1k(ones, __builtin_bit_cast(v4s, (v2u{a, b})), c, 0, 0, 0);
}
DEVI void wait_free(LAS char* sm, unsigned n) {
#pragma unroll 1
  for (unsigned i = 0; i < LSPIN_CAP; ++i) {
    v4u f = *(LAS volatile v4u*)(sm + S_FREE);
    unsigned m = f.x < f.y ? f.x : f.y, m2 = f.z < f.w ? f.z : f.w;
    if (__builtin_amdgcn_readfirstlane(m < m2 ? m : m2) >= n) break;
    __builtin_amdgcn_s_sleep(1);
  }
  MEMF();
}
DEVI void sig_set(LAS char* sm, int a, unsigned v) {
  asm volatile("s_mov_b64 exec, 1\n\tds_write_b32 %0, %1\n\ts_mov_b64 exec, -1" :: "v"((unsigned)(uintptr_t)(sm + a)), "v"(v) : "memory");
}
DEVI void sig_add(LAS char* sm, int a, int lane) {
  asm volatile("s_mov_b64 exec, 1\n\tds_add_u32 %0, %1\n\ts_mov_b64 exec, -1" :: "v"((unsigned)(uintptr_t)(sm + a)), "v"(1u) : "memory");
}
DEVI int wd_slot(int kh, int j) { return j < 8 ? L_R2 + (8 * kh + j) * 2048 : j < 18 ? L_R0 + (10 * kh + j - 8) * 2048 : L_R1 + (10 * kh + j - 18) * 2048; }

NOPK __global__ __launch_bounds__(512, 2) void fused_kernel(
    const u16* __restrict__ x, const u16* __restrict__ lar, const u16* __restrict__ aim, const u16* __restrict__ cre,
    const u16* __restrict__ cim, const u16* __restrict__ ldt, const u16* __restrict__ dsk, const u16* __restrict__ wg,
    const u16* __restrict__ wu, const u16* __restrict__ wd, u16* __restrict__ out, char* __restrict__ ws) {
  __shared__ __attribute__((aligned(16))) char smem[LDS_BYTES];
  LAS char* const sm = (LAS char*)smem;
  unsigned long long kpc;
  asm volatile("s_getpc_b64 %0" : "=s"(kpc));
  const unsigned lb = (unsigned)(uintptr_t)sm;
  const int bid = __builtin_amdgcn_workgroup_id_x(), tid = __builtin_amdgcn_workitem_id_x(), lane = tid & 63;
  const int w = __builtin_amdgcn_readfirstlane(tid >> 6);
  const int xcd = bid & 7, idx = bid >> 3;
  const bool csrole = idx >= 30;
  const int rb = (xcd >> 1) * 4 + (idx < 28 ? idx / 7 : (xcd & 1) * 2 + (idx & 1));
  const int cb = idx < 28 ? (xcd & 1) * 8 + idx % 7 : (idx < 30 ? 7 : imin((xcd & 1) * 8 + (xcd >> 1) * 2 + (idx & 1), 14));
  const int col0 = imin(cb * 96, 1280);
  const int cp = xcd * 32 + idx;
  const int tm = xcd * 4 + (idx >> 3), tn = idx & 7;
  const int hh = lane >> 5, l31 = lane & 31, l15 = lane & 15, g4 = lane >> 4;
  const unsigned oo = (unsigned)((tid >> 3) * 1024 + (tid & 7) * 16);

  if (w == 7) {
    __builtin_amdgcn_s_setprio(3);
    unsigned xv[4], gv[3], uv[3], dv[4];
#pragma unroll
    for (int j = 0; j < 4; ++j) {
      xv[j] = (lane >> 3) * 1024 + (((lane & 7) ^ ((4 * j + g4) & 7)) << 4) + j * 7168;
      dv[j] = (lane >> 3) * 1024 + (((lane & 7) ^ ((g4 & 1) << 2)) << 4) + j * 7168;
    }
#pragma unroll
    for (int j = 0; j < 3; ++j) {
      int p = 16 * j + (lane >> 2);
      gv[j] = (p / 3) * 2752 + (p % 3) * 64 + (lane & 3) * 16 - j * 1024;
      uv[j] = gv[j] ^ 32;
    }
    const char* xbase = (const char*)(x + (size_t)rb * 128 * 512);
    const char* gbase = (const char*)wg + col0 * 2;
    const char* ubase = (const char*)wu + col0 * 2;
    const int rot = ROT;
    unsigned slot = lb;
    const char* dbase = (const char*)wd + tn * 128;
#pragma unroll 1
    for (int s = 0; s < 8; ++s) {
      if (s == 1) BARRIER();
      if (s >= 3) wait_free(sm, s - 2);
      const int ks = (s + rot) & 7;
      const char* xb = xbase + ks * 128;
      const char* gb = gbase + ks * (64 * 2752);
      const char* ub = ubase + ks * (64 * 2752);
      if (!csrole) {
#pragma unroll
        for (int i = 0; i < 4; ++i) glds4(xv[0], xv[1], xv[2], xv[3], xb + i * 32768, slot + i * 4096);
      }
#pragma unroll
      for (int k = 0; k < 4; ++k) {
        glds3(gv[0], gv[1], gv[2], gb + k * (16 * 2752), slot + 16384 + k * 3072);
        glds3(uv[0], uv[1], uv[2], ub + k * (16 * 2752), slot + 28672 + k * 3072);
        if (s >= 1) {
          if (csrole) { WAITV(36); sig_set(sm, S_FULL, imax(4 * s + k - 5, 0)); }
          else { WAITV(40); sig_set(sm, S_FULL, 4 * (s - 1) + k + 1); }
        }
      }
      slot += SLOT; if (slot == lb + 3 * SLOT) slot = lb;
    }
    STAMP(24);
    if (csrole) { WAITV(0); sig_set(sm, S_FULL, 32); }
    wait_free(sm, 6);
    const unsigned f0 = csrole ? 32 : 29, fs = csrole ? 0 : 1;
    glds4(dv[0], dv[1], dv[2], dv[3], dbase, lb + wd_slot(0, 0)); glds4(dv[0], dv[1], dv[2], dv[3], dbase + 43 * 16384, lb + wd_slot(1, 0));
    WAITV(26); sig_set(sm, S_FULL, f0);
    glds4(dv[0], dv[1], dv[2], dv[3], dbase + 2 * 16384, lb + wd_slot(0, 2)); glds4(dv[0], dv[1], dv[2], dv[3], dbase + 45 * 16384, lb + wd_slot(1, 2));
    WAITV(28); sig_set(sm, S_FULL, f0 + fs);
    glds4(dv[0], dv[1], dv[2], dv[3], dbase + 4 * 16384, lb + wd_slot(0, 4)); glds4(dv[0], dv[1], dv[2], dv[3], dbase + 47 * 16384, lb + wd_slot(1, 4));
    WAITV(30); sig_set(sm, S_FULL, f0 + 2 * fs);
    glds4(dv[0], dv[1], dv[2], dv[3], dbase + 6 * 16384, lb + wd_slot(0, 6)); glds4(dv[0], dv[1], dv[2], dv[3], dbase + 49 * 16384, lb + wd_slot(1, 6));
    WAITV(32); sig_set(sm, S_FULL, 32);
    STAMP(25);
    wait_free(sm, 7);
#pragma unroll 1
    for (int j = 0; j < 10; j += 2) {
      glds4(dv[0], dv[1], dv[2], dv[3], dbase + (8 + j) * 16384, lb + L_R0 + j * 2048);
      glds4(dv[0], dv[1], dv[2], dv[3], dbase + (51 + j) * 16384, lb + L_R0 + (10 + j) * 2048);
    }
    WAITV(0); sig_set(sm, S_FULL, 33);
    STAMP(26);
    wait_ge(sm, S_STD, 4);
#pragma unroll 1
    for (int j = 0; j < 10; j += 2) {
      glds4(dv[0], dv[1], dv[2], dv[3], dbase + (18 + j) * 16384, lb + L_R1 + j * 2048);
      glds4(dv[0], dv[1], dv[2], dv[3], dbase + (61 + j) * 16384, lb + L_R1 + (10 + j) * 2048);
    }
    WAITV(0); sig_set(sm, S_FULL, 34);
    STAMP(27);
    __builtin_amdgcn_s_setprio(0);
  }
  unsigned tag;

  if (w < 4) {
    __builtin_amdgcn_s_setprio(2);
    if (w == 0 && lane < 32) *(LAS volatile unsigned*)(sm + L_SYNC + lane * 4) = 0u;
    unsigned warm = 0;
    {
      const char* pa = (const char*)x;
      bool on = false;
      if (w == 1) {
        on = lane < 40;
        pa = lane < 28 ? ws + (size_t)lane * (512u << 10) + 65536 + idx * 128 : lane < 32 ? (const char*)out + (lane - 28) * (512u << 10) + idx * 128
           : lane < 36 ? (const char*)wd + (lane - 32) * (344u << 10) + idx * 128 : (const char*)x + (lane - 36) * (512u << 10) + idx * 128;
      }
      if (on) asm volatile("global_load_dword %0, %1, off" : "+v"(warm) : "v"(pa) : "memory");
    }
    BARRIER();
    const int tb2 = w & 1, chf = w >> 1;
    int xa[4], wa[3];
    {
      int tok = 64 * tb2 + l31;
      int b0 = tok * 128 + ((hh ^ ((tok >> 1) & 7)) << 4);
#pragma unroll
      for (int s = 0; s < 4; ++s) xa[s] = b0 ^ (s << 5);
      int mat = g4 & 1, q = (lane >> 2) & 3, p = lane & 3;
#pragma unroll
      for (int t = 0; t < 3; ++t) {
        int T = 3 * chf + t;
        wa[t] = 16384 + mat * 12288 + (8 * hh + q) * 192 + (T >> 1) * 64 + (((T & 1) ^ mat) << 5) + p * 8;
      }
    }
    const unsigned fulladdr = lb + S_FULL;
    if (csrole) {
      f32x4 cs[3];
#pragma unroll
      for (int t = 0; t < 3; ++t) { cs[t] = f32x4{0.f, 0.f, 0.f, 0.f}; wa[t] += tb2 * 768; }
#pragma unroll 1
      for (int s = 0; s < 8; ++s) {
        wait_ge(sm, S_FULL, 4 * s + 4);
        v2u f[4][3];
#pragma unroll
        for (int k = 0; k < 4; ++k)
#pragma unroll
          for (int t = 0; t < 3; ++t) f[k][t] = LDTR(wa[t] + k * 3072);
        sig_set(sm, S_FREE + w * 4, s + 1);
#pragma unroll
        for (int k = 0; k < 4; ++k)
#pragma unroll
          for (int t = 0; t < 3; ++t) cs[t] = lanesum4(f[k][t].x, f[k][t].y, cs[t]);
        const int adv = (s == 2 || s == 5) ? -2 * SLOT : SLOT;
#pragma unroll
        for (int t = 0; t < 3; ++t) wa[t] += adv;
      }
#pragma unroll
      for (int t = 0; t < 3; ++t) *(LAS float*)(sm + L_CSX + ((w * 3 + t) * 64 + lane) * 4) = cs[t][0];
      sig_add(sm, S_HCNT, lane);
      wait_ge(sm, S_LN, 1);
      tag = lds_rd(sm, S_TAG);
      if (w < 3) {
        wait_ge(sm, S_HCNT, 4);
        const int T = 2 * w + hh, c2 = T >= 3 ? 1 : 0, t = T - 3 * c2;
        const LAS float* p = (const LAS float*)(sm + L_CSX) + ((2 * c2) * 3 + t) * 64 + l31;
        float c = (p[0] + p[32]) + (p[192] + p[224]);
        st8_sc1(v2u{__builtin_bit_cast(unsigned, c), tag}, (unsigned)((64 * w + lane) * 8), ws + WS_CS + cb * 2048);
      }
      STAMP(2);
      sig_add(sm, S_STD, lane);
    } else if (cb == 14 && chf == 0) {
      sig_set(sm, S_FREE + w * 4, 8);
      sig_add(sm, S_STD, lane);
      wait_ge(sm, S_LN, 1);
      tag = lds_rd(sm, S_TAG);
    } else {
      f32x16 acc[2][3];
#pragma unroll
      for (int b = 0; b < 2; ++b)
#pragma unroll
        for (int t = 0; t < 3; ++t)
#pragma unroll
          for (int r = 0; r < 16; ++r) acc[b][t][r] = 0.f;
      v4u x0[2], w0[3], x1[2], w1[3], x2[2], w2[3], x3[2], w3[3];
      unsigned fv;
#define G1_M(X, W, b, t) do { SCHED(); acc[b][t] = mfma32(W[t], X[b], acc[b][t]); SCHED(); } while (0)
#define G1_STEP(Xc, Wc, Xn, Wn, kn, need, EXTRA) do { \
        G1_M(Xc, Wc, 0, 0); \
        const unsigned fs_ = __builtin_amdgcn_readfirstlane(fv); \
        G1_M(Xc, Wc, 0, 1); \
        if (__builtin_expect(fs_ < (unsigned)(need), 0)) spin_ge(fulladdr, (need)); \
        MEMF(); Xn[0] = LD128(xa[kn]); \
        G1_M(Xc, Wc, 0, 2); \
        Wn[0] = trfrag(sm, wa[0] + (kn) * 3072, 768); \
        G1_M(Xc, Wc, 1, 0); \
        fv = *(LAS volatile unsigned*)(sm + S_FULL); \
        Wn[1] = trfrag(sm, wa[1] + (kn) * 3072, 768); \
        G1_M(Xc, Wc, 1, 1); \
        Wn[2] = trfrag(sm, wa[2] + (kn) * 3072, 768); \
        G1_M(Xc, Wc, 1, 2); \
        Xn[1] = LD128(xa[kn] + 4096); \
        xa[kn] += adv; \
        EXTRA; \
      } while (0)
      fv = spin_ge(fulladdr, 2);
      STAMP(1);
      x0[0] = LD128(xa[0]); x0[1] = LD128(xa[0] + 4096); x1[0] = LD128(xa[1]); x1[1] = LD128(xa[1] + 4096);
#pragma unroll
      for (int t = 0; t < 3; ++t) { w0[t] = trfrag(sm, wa[t], 768); w1[t] = trfrag(sm, wa[t] + 3072, 768); }
      xa[0] += SLOT; xa[1] += SLOT;
#pragma unroll 1
      for (int s = 0; s < 8; ++s) {
        int adv = (s == 2 || s == 5) ? -2 * SLOT : SLOT;
        G1_STEP(x0, w0, x2, w2, 2, 4 * s + 3, );
        G1_STEP(x1, w1, x3, w3, 3, 4 * s + 4, { sig_set(sm, S_FREE + w * 4, s + 1); wa[0] += adv; wa[1] += adv; wa[2] += adv; });
        adv = (s == 1 || s == 4) ? -2 * SLOT : SLOT;
        G1_STEP(x2, w2, x0, w0, 0, imin(4 * s + 5, 32), );
        G1_STEP(x3, w3, x1, w1, 1, imin(4 * s + 6, 32), );
      }
      STAMP(2);
      wait_ge(sm, S_CS, 1);
      tag = lds_rd(sm, S_TAG);
      STAMP(3);
      v4u am[3];
#pragma unroll
      for (int t = 0; t < 3; ++t) {
        v2u a = *(LAS const v2u*)(sm + L_CSX + ((3 * chf + t) * 32 + l31) * 8);
        am[t] = v4u{hh ? 0u : a.x, hh ? 0u : a.y, 0u, 0u};
      }
#pragma unroll
      for (int t = 0; t < 3; ++t)
#pragma unroll
        for (int b = 0; b < 2; ++b) {
          const unsigned nm = LD16(L_NM + (64 * tb2 + 32 * b + l31) * 2);
          acc[b][t] = mfma32(am[t], v4u{nm | (nm << 16), nm, 0u, 0u}, acc[b][t]);
        }
      char* hb = ws + WS_H + ((size_t)(4 * rb + 2 * tb2) * 86 + (col0 >> 4) + 3 * chf) * 1024;
#pragma unroll
      for (int t = 0; t < 3; ++t) {
#pragma unroll
        for (int b = 0; b < 2; ++b) {
          float hv[8];
#pragma unroll
          for (int j = 0; j < 8; ++j) hv[j] = __builtin_fmaxf(acc[b][t][j], 0.f) * acc[b][t][8 + j];
          v4u o = {pkbf(hv[0], hv[1]), pkbf(hv[2], hv[3]), pkbf(hv[4], hv[5]), pkbf(hv[6], hv[7])};
          if (t > 0 || cb < 14) st16_sc1(o, (unsigned)(lane * 16), hb + (b * 86 + t) * 1024);
        }
      }
      sig_add(sm, S_STD, lane);
      STAMP(4);
    }
    const int th = w & 1, kh = w >> 1;
    int da[2];
    {
      int q = (lane >> 2) & 3, p = lane & 3;
#pragma unroll
      for (int e = 0; e < 2; ++e) da[e] = (4 * hh + q) * 128 + ((e ^ (q >> 1)) << 6) + (g4 & 1) * 32 + p * 8;
    }
    wait_ge(sm, S_FULL, 33);
    v4u wr[15][2];
#pragma unroll
    for (int i = 0; i < 15; ++i)
#pragma unroll
      for (int e = 0; e < 2; ++e) wr[i][e] = trfrag(sm, wd_slot(kh, i) + da[e], 1024);
    sig_add(sm, S_PAIR + kh * 4, lane);
    STAMP(5);
    asm volatile("s_waitcnt vmcnt(0)" : "+v"(warm) :: "memory");
    if (!csrole) {
      unsigned old = 0;
      if (lane == 0) old = __hip_atomic_fetch_add((LAS unsigned*)(sm + S_HCNT), 1u, __ATOMIC_RELAXED, __HIP_MEMORY_SCOPE_WORKGROUP);
      if (__builtin_amdgcn_readfirstlane(old) == 3 && lane == 0) st4_sc1(tag, (unsigned)(cb * 8), ws + WS_F3 + rb * 128);
    }
    STAMP(6);
    wait_ge(sm, S_PAIR + kh * 4, 2);
    {
      const unsigned dvo = (8 * th + (lane >> 3)) * 1024 + (((lane & 7) ^ ((g4 & 1) << 2)) << 4);
      const char* db = (const char*)wd + tn * 128 + (size_t)(43 * kh + 28) * 16384;
#pragma unroll
      for (int i = 0; i < 15; ++i) glds1(dvo, db + i * 16384, lb + wd_slot(kh, i) + th * 1024);
    }
    WAITV(0);
    sig_add(sm, S_REC, lane);
    if (w == 0) {
      unsigned fl;
#pragma unroll 1
      for (unsigned spins = 0; spins < SPIN_CAP; ++spins) {
        ld4_sc1(fl, (unsigned)(imin(l15, 14) * 8), ws + WS_F3 + (tm >> 1) * 128);
        asm volatile("s_waitcnt vmcnt(0)" : "+v"(fl) :: "memory");
        if (allv(fl == tag)) break;
        __builtin_amdgcn_s_sleep(POLL_SLEEP);
      }
      sig_set(sm, S_HRDY, 1);
    } else {
      wait_ge(sm, S_HRDY, 1);
    }
    STAMP(7);
    constexpr int HW = 14;
    v4u hf[HW];
    const char* hbase = ws + WS_H + ((size_t)(2 * tm + th) * 86 + 43 * kh) * 1024;
    const unsigned hl = lane * 16;
#pragma unroll
    for (int i = 0; i < HW; ++i) ld16_sc1(hf[i], hl, hbase + i * 1024);
    f32x16 oa[2];
#pragma unroll
    for (int e = 0; e < 2; ++e)
#pragma unroll
      for (int r = 0; r < 16; ++r) oa[e][r] = 0.f;
    v4u wq[43][2];
#pragma unroll
    for (int i = 0; i < 15; ++i) { wq[i][0] = wr[i][0]; wq[i][1] = wr[i][1]; }
#pragma unroll
    for (int i = 0; i < 43; ++i) {
      constexpr int PD = 3;
      const int n = i + PD, j = n < 28 ? n : n - 28;
      if (n == 18) wait_ge(sm, S_FULL, 34);
      if (n == 28) wait_ge(sm, S_REC, 4);
      asm volatile("s_waitcnt vmcnt(%1)" : "+v"(hf[i % HW]) : "i"(i + HW <= 43 ? HW - 1 : 42 - i) : "memory");
      SCHED();
      oa[0] = mfma32(wq[i][0], hf[i % HW], oa[0]);
      SCHED();
      if (n >= 15 && n < 43) wq[n][0] = trfrag(sm, wd_slot(kh, j) + da[0], 1024);
      if (n >= 15 && n < 43) wq[n][1] = trfrag(sm, wd_slot(kh, j) + da[1], 1024);
      SCHED();
      oa[1] = mfma32(wq[i][1], hf[i % HW], oa[1]);
      SCHED();
      if (i + HW < 43) ld16_sc1(hf[i % HW], hl, hbase + (i + HW) * 1024);
    }
    STAMP(8);
    wait_ge(sm, S_CONV, 4);
#pragma unroll
    for (int e = 0; e < 2; ++e)
#pragma unroll
      for (int i = 0; i < 4; ++i)
        *(LAS f32x4*)(sm + L_RED + kh * REDQ + (32 * th + l31) * REDP + (32 * e + 8 * i + 4 * hh) * 4) =
            f32x4{oa[e][4 * i], oa[e][4 * i + 1], oa[e][4 * i + 2], oa[e][4 * i + 3]};
    STAMP(9);
  } else {
    const unsigned dsk2 = ((const unsigned*)dsk)[cp];
    if (w < 6) {
      const int ch = w - 4, kd = 2 * cp + ch;
      unsigned la, ai, cr, ci;
      ld4(la, (unsigned)(lane * 4), lar + kd * 128); ld4(ai, (unsigned)(lane * 4), aim + kd * 128);
      ld4(cr, (unsigned)(lane * 4), cre + kd * 128); ld4(ci, (unsigned)(lane * 4), cim + kd * 128);
      const unsigned ldt2 = ((const unsigned*)ldt)[cp];
      BARRIER();
      const float dt = rbf(expacc(ch ? bfhi(ldt2) : bflo(ldt2)));
      asm volatile("s_waitcnt vmcnt(0)" : "+v"(la), "+v"(ai), "+v"(cr), "+v"(ci) :: "memory");
      STAMP(12);
      int mx = imax(imax((int)(cr & 0x7fff), (int)((cr >> 16) & 0x7fff)), imax((int)(ci & 0x7fff), (int)((ci >> 16) & 0x7fff)));
      const int ex = imin(imax(wave_maxi(mx) >> 7, 1), 253);
      const float sc = __builtin_bit_cast(float, (unsigned)(254 - ex) << 23);
      const float kscale = 2.0f * dt * __builtin_bit_cast(float, (unsigned)ex << 23);
      float ar2[2], ai2[2]; cf c0[2];
      ar2[0] = -rbf(expacc(bflo(la))) * dt * 1.44269504088896341f; ar2[1] = -rbf(expacc(bfhi(la))) * dt * 1.44269504088896341f;
      ai2[0] = bflo(ai) * dt * 0.15915494309189535f; ai2[1] = bfhi(ai) * dt * 0.15915494309189535f;
      c0[0].r = bflo(cr) * sc; c0[0].i = bflo(ci) * sc; c0[1].r = bfhi(cr) * sc; c0[1].i = bfhi(ci) * sc;
      const int kb = L_KC + ch * KCCH, kw = kb + lane * 8, kr = kb + l15 * KGP + g4 * 16;
      const int tb = L_TBL + ch * TBLCH;
      auto chain = [&](cf (&z)[2], const cf (&m)[2], int n, int row0, int rstep) NOPK __attribute__((always_inline)) {
#pragma unroll
        for (int i = 0; i < n; ++i) {
          ST64(kw + (row0 + i * rstep) * KGP, (v2u{pkh(z[0].r, z[0].i), pkh(z[1].r, z[1].i)}));
          z[0] = cmul(z[0], m[0]); z[1] = cmul(z[1], m[1]);
        }
      };
      cf m[2], m128[2], z[2], g[2];
      v4u yf[8];
      m[0] = cexpp(ar2[0], -ai2[0], 8.f); m[1] = cexpp(ar2[1], -ai2[1], 8.f);
      z[0].r = 1.f; z[0].i = 0.f; z[1] = z[0];
      chain(z, m, 16, 0, 1);
#pragma unroll
      for (int s = 0; s < 8; ++s) yf[s] = LD128(kr + s * 64);
      MEMF();
      v4u xf[3][8];
#define KG_GRAB(t) do { MEMF(); _Pragma("unroll") for (int s = 0; s < 8; ++s) xf[t][s] = LD128(kr + s * 64); MEMF(); } while (0)
      m128[0] = cexpp(ar2[0], ai2[0], 128.f); m128[1] = cexpp(ar2[1], ai2[1], 128.f);
      m[0] = cexpp(ar2[0], ai2[0], 1.f); m[1] = cexpp(ar2[1], ai2[1], 1.f);
      z[0] = c0[0]; z[1] = c0[1];
      chain(z, m, 8, 0, 1);
      g[0] = cmul(c0[0], m128[0]); g[1] = cmul(c0[1], m128[1]); z[0] = g[0]; z[1] = g[1];
      chain(z, m, 8, 8, 1);
      KG_GRAB(0);
      m[0] = cmul(m[0], m[0]); m[1] = cmul(m[1], m[1]);
      g[0] = cmul(g[0], m128[0]); g[1] = cmul(g[1], m128[1]); z[0] = g[0]; z[1] = g[1];
      chain(z, m, 4, 0, 1);
      g[0] = cmul(g[0], m128[0]); g[1] = cmul(g[1], m128[1]); z[0] = g[0]; z[1] = g[1];
      chain(z, m, 4, 4, 1);
      m[0] = cmul(m[0], m[0]); m[1] = cmul(m[1], m[1]);
      g[0] = cmul(g[0], m128[0]); g[1] = cmul(g[1], m128[1]);
      z[0] = cmul(g[0], m[0]); z[1] = cmul(g[1], m[1]);
      chain(z, m128, 4, 9, 2);
      chain(g, m128, 4, 8, 2);
      KG_GRAB(1);
      chain(g, m128, 9, 0, 1);
      KG_GRAB(2);
      STAMP(22);
      {
        f32x4 a[3];
#pragma unroll
        for (int t = 0; t < 3; ++t) a[t] = f32x4{0.f, 0.f, 0.f, 0.f};
#pragma unroll
        for (int s = 0; s < 8; ++s)
#pragma unroll
          for (int t = 0; t < 3; ++t) a[t] = mfma16h(xf[t][s], yf[s], a[t]);
        ST64(tb + 2 * (128 * (g4 >> 1) + 8 * l15 + 4 * (g4 & 1)), (v2u{pkbf(a[0][0] * kscale, a[0][1] * kscale), pkbf(a[0][2] * kscale, a[0][3] * kscale)}));
        const int i0 = g4 < 2 ? 256 + 64 * g4 + 4 * l15 : 384 + 64 * (g4 - 2) + 2 * l15, i2 = g4 < 2 ? i0 + 2 : i0 + 32;
        *(LAS unsigned*)(sm + tb + 2 * i0) = pkbf(a[1][0] * kscale, a[1][1] * kscale);
        *(LAS unsigned*)(sm + tb + 2 * i2) = pkbf(a[1][2] * kscale, a[1][3] * kscale);
        const int i3 = tb + 2 * (512 + 64 * g4 + l15);
        const unsigned p01 = pkbf(a[2][0] * kscale, a[2][1] * kscale), p23 = pkbf(a[2][2] * kscale, a[2][3] * kscale);
        ST16(i3, p01); ST16(i3 + 32, p01 >> 16); ST16(i3 + 64, p23); ST16(i3 + 96, p23 >> 16);
        MEMF();
      }
      STAMP(13);
      wait_ge(sm, S_LN, 1);
      {
        v4u d[4];
        const int nl = 63 - lane;
#pragma unroll
        for (int r = 0; r < 2; ++r) {
          const int a = tb + 2 * (512 + 64 * (1 - r) + nl);
          unsigned lo = LD16(a), hi = LD16(a + 2);
          unsigned c = (nl & 1) ? hi : lo;
          d[r] = v4u{hi | (hi << 16), hi | (c << 16), lo | (lo << 16), lo | (lo << 16)};
        }
        {
          const int a = tb + 2 * (384 + 2 * nl);
          unsigned w0 = *(LAS const unsigned*)(sm + a), p = LD16(a + 4);
          d[2] = v4u{p | (p << 16), __builtin_amdgcn_perm(w0, w0, 0x03020302u), __builtin_amdgcn_perm(w0, w0, 0x01000302u), __builtin_amdgcn_perm(w0, w0, 0x01000100u)};
        }
        {
          const bool s1 = lane < 32;
          const int a = tb + (s1 ? 2 * (256 + 4 * (nl - 32)) : 16 * nl);
          v2u wl = *(LAS const v2u*)(sm + a), wh = *(LAS const v2u*)(sm + a + 8);
          d[3].x = s1 ? __builtin_amdgcn_perm(wl.y, wh.x, 0x07060100u) : __builtin_amdgcn_perm(wh.y, wh.y, 0x01000302u);
          d[3].y = s1 ? __builtin_amdgcn_perm(wl.y, wl.y, 0x01000100u) : __builtin_amdgcn_perm(wh.x, wh.x, 0x01000302u);
          d[3].z = s1 ? __builtin_amdgcn_perm(wl.x, wl.y, 0x07060100u) : __builtin_amdgcn_perm(wl.y, wl.y, 0x01000302u);
          d[3].w = __builtin_amdgcn_perm(wl.x, wl.x, s1 ? 0x01000100u : 0x01000302u);
        }
        const int cbs = kb + lane * 16;
#pragma unroll
        for (int r = 0; r < 4; ++r) ST128(cbs + r * 1024, d[r]);
        if (lane < 8) ST128(kb + (lane >> 1) * KCP + 4096 + (lane & 1) * 16, (v4u{0u, 0u, 0u, 0u}));
        MEMF();
#pragma unroll
        for (int r = 0; r < 4; ++r) {
          v2u nx = *(LAS const v2u*)(sm + cbs + r * 1024 + 16);
          unsigned o0 = __builtin_amdgcn_alignbit(d[r].y, d[r].x, 16), o1 = __builtin_amdgcn_alignbit(d[r].z, d[r].y, 16),
                   o2 = __builtin_amdgcn_alignbit(d[r].w, d[r].z, 16), o3 = __builtin_amdgcn_alignbit(nx.x, d[r].w, 16),
                   o4 = __builtin_amdgcn_alignbit(nx.y, nx.x, 16);
          ST128(cbs + r * 1024 + KCP, (v4u{o0, o1, o2, o3}));
          ST128(cbs + r * 1024 + 2 * KCP, (v4u{d[r].y, d[r].z, d[r].w, nx.x}));
          ST128(cbs + r * 1024 + 3 * KCP, (v4u{o1, o2, o3, o4}));
        }
      }
      sig_add(sm, S_KRDY, lane);
      STAMP(14);
    } else if (w == 6) {
      v4u xv[8];
      unsigned ev; ld4_sc1(ev, 0u, ws + WS_E);
      const int r = lane >> 3, c = lane & 7;
#pragma unroll
      for (int i = 0; i < 8; ++i) ld16(xv[i], (unsigned)(r * 1024 + c * 16 + i * 128), x + (size_t)bid * 4096);
      BARRIER();
      asm volatile("s_waitcnt vmcnt(0)" : "+v"(ev), "+v"(xv[0]), "+v"(xv[1]), "+v"(xv[2]), "+v"(xv[3]), "+v"(xv[4]), "+v"(xv[5]), "+v"(xv[6]), "+v"(xv[7]) :: "memory");
      tag = __builtin_amdgcn_readfirstlane(ev) + 0x9E3779B9u;
      STAMP(18);
      float v[64];
      float s0 = 0.f, s1 = 0.f, s2 = 0.f, s3 = 0.f;
#pragma unroll
      for (int i = 0; i < 8; ++i) {
#pragma unroll
        for (int j = 0; j < 4; ++j) { v[8 * i + 2 * j] = bflo(xv[i][j]); v[8 * i + 2 * j + 1] = bfhi(xv[i][j]); }
        s0 += v[8 * i] + v[8 * i + 4]; s1 += v[8 * i + 1] + v[8 * i + 5]; s2 += v[8 * i + 2] + v[8 * i + 6]; s3 += v[8 * i + 3] + v[8 * i + 7];
      }
      const float mean = sum8((s0 + s1) + (s2 + s3)) * (1.0f / 512);
      const float m = rbf(mean);
      s0 = s1 = s2 = s3 = 0.f;
#pragma unroll
      for (int i = 0; i < 64; i += 4) {
        v[i] -= m; v[i + 1] -= m; v[i + 2] -= m; v[i + 3] -= m;
        s0 += v[i] * v[i]; s1 += v[i + 1] * v[i + 1]; s2 += v[i + 2] * v[i + 2]; s3 += v[i + 3] * v[i + 3];
      }
      const float dm = mean - m;
      const float var = rbf(__builtin_fmaxf(sum8((s0 + s1) + (s2 + s3)) * (1.0f / 512) - dm * dm, 0.f));
      const float sd = rbf(__builtin_amdgcn_sqrtf(rbf(var + 1e-5f)));
      const float rs = __builtin_amdgcn_rcpf(sd);
      const int lw = L_KC + (r >> 2) * KCCH + LNOFF + (r & 3) * LNP + c * 16;
#pragma unroll
      for (int i = 0; i < 8; ++i) {
        v4u o;
#pragma unroll
        for (int j = 0; j < 4; ++j) { unsigned d = pkbf(v[8 * i + 2 * j], v[8 * i + 2 * j + 1]); o[j] = pkbf(bflo(d) * rs, bfhi(d) * rs); }
        ST128(lw + i * 128, o);
      }
      MEMF();
      {
        v2u sg = {pkbf(-m, sd), tag};
        if (c == 0) st8_sc1(sg, (unsigned)(r * 8), ws + WS_STAT + bid * 64);
        const int q = (lane >> 2) & 3, p = lane & 3;
        const int ta = L_KC + (p & 1) * KCCH + LNOFF + q * LNP + (2 * g4 + (p >> 1)) * 8;
#pragma unroll
        for (int i = 0; i < 16; ++i) {
          v2u t = LDTR(ta + i * 64);
          st16_sc1(v4u{t.x, tag, t.y, tag}, (unsigned)(lane * 16), ws + WS_XG + (size_t)bid * 16384 + i * 1024);
        }
      }
      sig_set(sm, S_TAG, tag);
      sig_set(sm, S_LN, 1);
      STAMP(19);
      wait_free(sm, 5);
      v4u ug[16], sg2;
      const unsigned ugo = (unsigned)((lane >> 2) * 16384 + ((lane >> 1) & 1) * 64 + (lane & 1) * 16);
      const char* ugbase = ws + WS_XG + (cp >> 1) * 128 + (cp & 1) * 32;
#pragma unroll 1
      for (unsigned spins = 0; spins < SPIN_CAP; ++spins) {
#pragma unroll
        for (int i = 0; i < 16; ++i) ld16_sc1(ug[i], ugo, ugbase + i * (16 * 16384));
        ld16_sc1(sg2, (unsigned)(lane * 16), ws + WS_STAT + rb * 1024);
        asm volatile("s_waitcnt vmcnt(0)" : "+v"(ug[0]), "+v"(ug[1]), "+v"(ug[2]), "+v"(ug[3]), "+v"(ug[4]), "+v"(ug[5]), "+v"(ug[6]), "+v"(ug[7]),
                     "+v"(ug[8]), "+v"(ug[9]), "+v"(ug[10]), "+v"(ug[11]), "+v"(ug[12]), "+v"(ug[13]), "+v"(ug[14]), "+v"(ug[15]), "+v"(sg2) :: "memory");
        unsigned bad = (sg2.y ^ tag) | (sg2.w ^ tag);
#pragma unroll
        for (int i = 0; i < 16; ++i) bad |= (ug[i].y ^ tag) | (ug[i].w ^ tag);
        if (allv(bad == 0)) break;
        __builtin_amdgcn_s_sleep(8);
      }
      STAMP(20);
      if (bid == 0 && lane == 0) st4_sc1(tag, 0u, ws + WS_E);
      *(LAS unsigned*)(sm + L_NM + lane * 4) = (sg2.x & 0xffffu) | (sg2.z << 16);
      wait_free(sm, 6);
      const int ua = L_U + (lane & 1) * UCH + 480 + (lane >> 2) * 16 + ((lane >> 1) & 1) * 8;
#pragma unroll
      for (int i = 0; i < 16; ++i) ST64(ua + i * 256, (v2u{ug[i].x, ug[i].z}));
      if (lane < 60) ST128(L_U + (lane / 30) * UCH + (lane % 30) * 16, (v4u{0u, 0u, 0u, 0u}));
      sig_set(sm, S_URDY, 1);
      STAMP(21);
      if (!csrole) {
        v2u cg[3];
#pragma unroll 1
        for (unsigned spins = 0; spins < SPIN_CAP; ++spins) {
#pragma unroll
          for (int i = 0; i < 3; ++i) ld8_sc1(cg[i], (unsigned)(lane * 8), ws + WS_CS + cb * 2048 + i * 512);
          asm volatile("s_waitcnt vmcnt(0)" : "+v"(cg[0]), "+v"(cg[1]), "+v"(cg[2]) :: "memory");
          if (allv(((cg[0].y ^ tag) | (cg[1].y ^ tag) | (cg[2].y ^ tag)) == 0)) break;
          __builtin_amdgcn_s_sleep(4);
        }
#pragma unroll
        for (int i = 0; i < 3; ++i) {
          float c = __builtin_bit_cast(float, cg[i].x);
          float c0 = rbf(c), c1 = rbf(c - c0), c2 = (c - c0) - c1;
          ST64(L_CSX + (64 * i + lane) * 8, (v2u{pkbf(c0, c1), pkbf(c2, 0.f)}));
        }
        sig_set(sm, S_CS, 1);
        STAMP(23);
      }
    }
    wait_ge(sm, S_URDY, 1);
    wait_ge(sm, S_KRDY, 2);
    tag = lds_rd(sm, S_TAG);
    if (w == 4) STAMP(15);
    const int cch = w & 1, ctg = (w >> 1) & 1;
    f32x4 ca[4];
#pragma unroll
    for (int i = 0; i < 4; ++i) ca[i] = f32x4{0.f, 0.f, 0.f, 0.f};
    {
      int ka = L_KC + cch * KCCH + ((~l15) & 3) * KCP + 2 * (2028 + 8 * g4 - 4 * (l15 >> 2)) - 7 * 512;
      int ub = L_U + cch * UCH + 480 + 32 * (l15 - 1) + 16 * g4;
#define CV_LOAD(A, B, n) do { _Pragma("unroll") for (int q = 0; q < n; ++q) { \
          v2u p0 = LD64V(ka + (7 - q) * 512), p1 = LD64V(ka + (7 - q) * 512 + 8); \
          A[q] = v4u{p0.x, p0.y, p1.x, p1.y}; B[q] = LD128(ub + q * 512); } ka -= 64; ub -= 64; } while (0)
#define CV_MMA0(A, B) do { ca[0] = mfma16(A[0], B[0], ca[0]); ca[1] = mfma16(A[0], B[1], ca[1]); ca[1] = mfma16(A[1], B[0], ca[1]); \
          _Pragma("unroll") for (int q = 0; q < 7; ++q) { ca[2] = mfma16(A[q], B[6 - q], ca[2]); ca[3] = mfma16(A[q], B[7 - q], ca[3]); } \
          ca[3] = mfma16(A[7], B[0], ca[3]); } while (0)
#define CV_MMA1(A, B) do { _Pragma("unroll") for (int t = 2; t < 6; ++t) _Pragma("unroll") for (int q = 0; q <= t; ++q) \
          ca[t - 2] = mfma16(A[q], B[t - q], ca[t - 2]); } while (0)
#define CV_IL(n, k) do { _Pragma("unroll") for (int z = 0; z < n; ++z) { __builtin_amdgcn_sched_group_barrier(0x8, 1, 0); \
          __builtin_amdgcn_sched_group_barrier(0x100, k, 0); } __builtin_amdgcn_sched_group_barrier(0x8, 18 - n, 0); } while (0)
      if (ctg == 0) {
        v4u A0[8], B0[8], A1[8], B1[8];
        CV_LOAD(A0, B0, 8);
#pragma unroll 1
        for (int it = 0; it < 4; ++it) {
          SCHED(); CV_LOAD(A1, B1, 8); CV_MMA0(A0, B0); CV_IL(12, 2); SCHED();
          { const int bk = it == 3 ? 64 : 0; ka += bk; ub += bk; }
          SCHED(); CV_LOAD(A0, B0, 8); CV_MMA0(A1, B1); CV_IL(12, 2); SCHED();
        }
      } else {
        v4u A0[6], B0[6], A1[6], B1[6];
        CV_LOAD(A0, B0, 6);
#pragma unroll 1
        for (int it = 0; it < 4; ++it) {
          SCHED(); CV_LOAD(A1, B1, 6); CV_MMA1(A0, B0); CV_IL(9, 2); SCHED();
          { const int bk = it == 3 ? 64 : 0; ka += bk; ub += bk; }
          SCHED(); CV_LOAD(A0, B0, 6); CV_MMA1(A1, B1); CV_IL(9, 2); SCHED();
        }
      }
    }
    {
      const float dk = cch ? bfhi(dsk2) : bflo(dsk2);
      const int lo = 16 * l15 + 4 * g4;
      char* yb = ws + WS_YG + (size_t)(2 * cp + cch) * 8192;
#pragma unroll
      for (int i = 0; i < 4; ++i) {
        const int t = ctg ? 2 + i : (i < 2 ? i : 4 + i);
        v2u u = *(LAS const v2u*)(sm + L_U + cch * UCH + 480 + (256 * t + lo) * 2);
        f32x4 y = ca[i];
        unsigned y01 = pkbf(y.x, y.y), y23 = pkbf(y.z, y.w);
        unsigned s01 = pkbf(bflo(u.x) * dk, bfhi(u.x) * dk), s23 = pkbf(bflo(u.y) * dk, bfhi(u.y) * dk);
        v4u o = {pkbf(bflo(y01) + bflo(s01), bfhi(y01) + bfhi(s01)), tag, pkbf(bflo(y23) + bflo(s23), bfhi(y23) + bfhi(s23)), tag};
        st16_sc1(o, (unsigned)(lo * 4), yb + t * 1024);
      }
    }
    sig_add(sm, S_CONV, lane);
    if (w == 4) STAMP(16);
    {
      const int xw = w - 4;
      v4u xr[2], yg[4]; v2u sg;
      const unsigned xo = (unsigned)((8 * xw + (lane >> 3)) * 1024 + (lane & 7) * 16);
      const char* xb = (const char*)x + (size_t)tm * 65536 + tn * 128;
      ld16(xr[0], xo, xb); ld16(xr[1], xo, xb + 32768);
      const unsigned ygo = (unsigned)((16 * xw + g4) * 8192 + l15 * 16);
      const char* ygbase = ws + WS_YG + (size_t)tn * 64 * 8192 + tm * 256;
#pragma unroll 1
      for (unsigned spins = 0; spins < SPIN_CAP; ++spins) {
#pragma unroll
        for (int i = 0; i < 4; ++i) ld16_sc1(yg[i], ygo, ygbase + i * 4 * 8192);
        ld8_sc1(sg, (unsigned)(lane * 8), ws + WS_STAT + tm * 512);
        asm volatile("s_waitcnt vmcnt(0)" : "+v"(yg[0]), "+v"(yg[1]), "+v"(yg[2]), "+v"(yg[3]), "+v"(sg), "+v"(xr[0]), "+v"(xr[1]) :: "memory");
        unsigned bad = sg.y ^ tag;
#pragma unroll
        for (int i = 0; i < 4; ++i) bad |= (yg[i].y ^ tag) | (yg[i].w ^ tag);
        if (allv(bad == 0)) break;
        __builtin_amdgcn_s_sleep(8);
      }
      wait_ge(sm, S_CONV, 4);
#pragma unroll
      for (int i = 0; i < 4; ++i) {
        const int ab = L_AIMG + (4 * l15) * 128 + (((2 * xw + (i >> 1)) ^ (l15 & 7)) << 4) + ((i & 1) * 4 + g4) * 2;
        ST16(ab, yg[i].x); ST16(ab + 128, yg[i].x >> 16); ST16(ab + 256, yg[i].z); ST16(ab + 384, yg[i].z >> 16);
      }
      if (xw == 0) { float rs = __builtin_amdgcn_rcpf(bfhi(sg.x)); *(LAS float*)(sm + L_RS2 + lane * 4) = rs * rs; }
      sig_add(sm, S_AIMG, lane);
      wait_ge(sm, S_AIMG, 4);
#pragma unroll
      for (int i = 0; i < 2; ++i) {
        const int tok = 32 * i + 8 * xw + (lane >> 3);
        const int a = L_AIMG + tok * 128 + (((lane & 7) ^ ((tok >> 2) & 7)) << 4);
        v4u av = LD128(a), o;
#pragma unroll
        for (int j = 0; j < 4; ++j) o[j] = pkbf(bflo(xr[i][j]) + bflo(av[j]), bfhi(xr[i][j]) + bfhi(av[j]));
        ST128(a, o);
      }
      if (w == 4) STAMP(17);
    }
  }
  BARRIER();
  if (w == 0) STAMP(10);
  {
    const int tok = tid >> 3, c = tid & 7;
    const LAS f32x4* rp = (const LAS f32x4*)(sm + L_RED + tok * REDP + c * 32);
    f32x4 s0 = rp[0] + rp[REDQ / 16], s1 = rp[1] + rp[REDQ / 16 + 1];
    const float rs2 = *(LAS const float*)(sm + L_RS2 + tok * 4);
    v4u rv = LD128(L_AIMG + tok * 128 + ((c ^ ((tok >> 2) & 7)) << 4));
    unsigned g0 = pkbf(s0.x * rs2, s0.y * rs2), g1 = pkbf(s0.z * rs2, s0.w * rs2), g2 = pkbf(s1.x * rs2, s1.y * rs2), g3 = pkbf(s1.z * rs2, s1.w * rs2);
    v4u o = {pkbf(bflo(rv.x) + bflo(g0), bfhi(rv.x) + bfhi(g0)), pkbf(bflo(rv.y) + bflo(g1), bfhi(rv.y) + bfhi(g1)),
             pkbf(bflo(rv.z) + bflo(g2), bfhi(rv.z) + bfhi(g2)), pkbf(bflo(rv.w) + bflo(g3), bfhi(rv.w) + bfhi(g3))};
    st16(o, oo, (char*)out + (size_t)tm * 65536 + tn * 128);
  }
}

extern "C" void kernel_launch(void* const* d_in, const int* in_sizes, int n_in,
                              void* d_out, int out_size, void* d_ws, size_t ws_size,
                              hipStream_t stream) {
  fused_kernel<<<256, 512, 0, stream>>>(
      (const u16*)d_in[0], (const u16*)d_in[1], (const u16*)d_in[2], (const u16*)d_in[3], (const u16*)d_in[4],
      (const u16*)d_in[5], (const u16*)d_in[6], (const u16*)d_in[8], (const u16*)d_in[9], (const u16*)d_in[10],
      (u16*)d_out, (char*)d_ws);
}
```

```cpp
#include <hip/hip_runtime.h>
#include <hip/hip_bf16.h>
#include <stdint.h>

#define F3S 256
#define F4S 256
#define F4SLEEP 8
#define PNT " nt"
#define CSLAG 48
#define SC_AT 5
#define G2PRIO "0"
#define CODE_BYTES 28160
#define LPRIO 1
#define LPRIO2 0
#define CPRIO 1
#define MPRIO 2
#define XPRIO 1
#define GATHER_AT 3
#define POLLSLEEP "1"
typedef unsigned short u16;
typedef unsigned v4u __attribute__((ext_vector_type(4)));
typedef unsigned v2u __attribute__((ext_vector_type(2)));
typedef short v4s __attribute__((ext_vector_type(4)));
typedef float f32x2 __attribute__((ext_vector_type(2)));
typedef float f32x4 __attribute__((ext_vector_type(4)));
typedef float f32x16 __attribute__((ext_vector_type(16)));
typedef __bf16 bf16x2 __attribute__((ext_vector_type(2)));
typedef _Float16 f16x2 __attribute__((ext_vector_type(2)));
typedef _Float16 f16x8 __attribute__((ext_vector_type(8)));
#define LAS __attribute__((address_space(3)))
#if defined(__HIP_DEVICE_COMPILE__)
#define NOPK __attribute__((target("no-packed-fp32-ops")))
#else
#define NOPK
#endif
#define DEVI NOPK __device__ __forceinline__

constexpr size_t WS_E = 0;
constexpr size_t WS_STAT = 16384;
constexpr size_t WS_CS = 32768;
constexpr size_t WS_F3 = 65536;
constexpr size_t WS_F4 = 131072;
constexpr size_t WS_YG = 262144;
constexpr size_t WS_H = WS_YG + (2u << 20);

constexpr int SLOT = 40960;
constexpr int L_WD = 0;
constexpr int L_KG = 122880, KGP = 528, KGCH = 16 * KGP;
constexpr int L_U = L_KG + 2 * KGCH, UP = 1168, UCH = 4 * UP;
constexpr int LNP = 1088;
constexpr int L_KX = L_U + 2 * UCH, KXCH = 4160;
constexpr int L_TBL = L_KX + 2 * KXCH - 512, TBLCH = 1024;
constexpr int L_MB = L_KX + 2 * KXCH + 2048;
static_assert(L_MB + 288 <= 160256, "");
constexpr int L_CSX = 160256;
constexpr int L_RED = L_WD, REDP = 272, REDQ = 64 * REDP;
constexpr int L_AIMG = L_KG + 4096, L_XIMG = 114688;
constexpr int L_NM = L_CSX + 2048;
constexpr int L_SYNC = 163584, L_SD = 163712;
constexpr int S_FULL = L_SYNC, S_KRDY = L_SYNC + 8, S_URDY = L_SYNC + 12, S_FREE = L_SYNC + 16,
              S_HCNT = L_SYNC + 32, S_YCNT = L_SYNC + 88, S_WDONE = L_SYNC + 44, S_CONV = L_SYNC + 48,
              S_TAG = L_SYNC + 56, S_CS = L_SYNC + 60, S_LNW = L_SYNC + 64, S_HOK = L_SYNC + 68, S_PRE = L_SYNC + 72, S_REC = L_SYNC + 76,
              S_CSF = L_SYNC + 80, S_TAGOK = L_SYNC + 84, L_STW = L_SYNC + 96;
#define W42OFF "0x1e000"
static_assert(L_KG - L_WD == 0x1e000, "");
constexpr int LDS_BYTES = 163840;

constexpr unsigned SPIN_CAP = 1u << 16, LSPIN_CAP = 1u << 22;

#define BARRIER() asm volatile("s_waitcnt lgkmcnt(0)\n\ts_barrier" ::: "memory")
#define WAITV(n) asm volatile("s_waitcnt vmcnt(" #n ")" ::: "memory")
#define SCHED() __builtin_amdgcn_sched_barrier(0)
#define MEMF() asm volatile("" ::: "memory")

DEVI void glds8(const unsigned (&v)[8], const void* sb, unsigned lds) {
  asm volatile("s_mov_b32 m0, %9\n\ts_nop 0\n\tglobal_load_lds_dwordx4 %0, %8 offset:-4096\n\tglobal_load_lds_dwordx4 %1, %8 offset:-3072\n\t"
               "global_load_lds_dwordx4 %2, %8 offset:-2048\n\tglobal_load_lds_dwordx4 %3, %8 offset:-1024\n\tglobal_load_lds_dwordx4 %4, %8\n\t"
               "global_load_lds_dwordx4 %5, %8 offset:1024\n\tglobal_load_lds_dwordx4 %6, %8 offset:2048\n\tglobal_load_lds_dwordx4 %7, %8 offset:3072"
               :: "v"(v[0]), "v"(v[1]), "v"(v[2]), "v"(v[3]), "v"(v[4]), "v"(v[5]), "v"(v[6]), "v"(v[7]), "s"(sb), "s"(lds) : "memory");
}
DEVI void glds6(const unsigned (&g)[3], const unsigned (&u)[3], const void* gb, const void* ub, unsigned lds) {
  asm volatile("s_mov_b32 m0, %8\n\ts_nop 0\n\tglobal_load_lds_dwordx4 %0, %6 offset:-3072\n\tglobal_load_lds_dwordx4 %1, %6 offset:-2048\n\t"
               "global_load_lds_dwordx4 %2, %6 offset:-1024\n\tglobal_load_lds_dwordx4 %3, %7\n\t"
               "global_load_lds_dwordx4 %4, %7 offset:1024\n\tglobal_load_lds_dwordx4 %5, %7 offset:2048"
               :: "v"(g[0]), "v"(g[1]), "v"(g[2]), "v"(u[0]), "v"(u[1]), "v"(u[2]), "s"(gb), "s"(ub), "s"(lds) : "memory");
}
template <int N> DEVI void publish(unsigned addr, unsigned& cnt) {
  asm volatile("v_add_u32 %0, 1, %0\n\ts_mov_b64 exec, 1\n\ts_waitcnt vmcnt(%2)\n\tds_max_u32 %1, %0\n\ts_mov_b64 exec, -1" : "+v"(cnt) : "v"(addr), "i"(N) : "memory");
}
DEVI int rd_free(LAS char* sm) {
  v4u f = *(LAS volatile v4u*)(sm + S_FREE);
  unsigned m = f.x < f.y ? f.x : f.y, m2 = f.z < f.w ? f.z : f.w;
  return __builtin_amdgcn_readfirstlane(m < m2 ? m : m2);
}
DEVI void gldsp(unsigned v0, unsigned v1, unsigned v2, unsigned v3, const void* b0, const void* b1, unsigned lds) {
  asm volatile("s_mov_b32 m0, %6\n\ts_nop 0\n\tglobal_load_lds_dwordx4 %0, %4\n\tglobal_load_lds_dwordx4 %1, %4 offset:1024\n\t"
               "global_load_lds_dwordx4 %2, %5 offset:2048\n\tglobal_load_lds_dwordx4 %3, %5 offset:3072"
               :: "v"(v0), "v"(v1), "v"(v2), "v"(v3), "s"(b0), "s"(b1), "s"(lds) : "memory");
}
DEVI void ld16_sc1(v4u& d, unsigned voff, const void* sbase) {
  asm volatile("global_load_dwordx4 %0, %1, %2 sc1" : "=v"(d) : "v"(voff), "s"(sbase) : "memory");
}
DEVI void ld16(v4u& d, unsigned voff, const void* sbase) {
  asm volatile("global_load_dwordx4 %0, %1, %2" : "=v"(d) : "v"(voff), "s"(sbase) : "memory");
}
DEVI void ld8_sc1(v2u& d, unsigned voff, const void* sbase) {
  asm volatile("global_load_dwordx2 %0, %1, %2 sc1" : "=v"(d) : "v"(voff), "s"(sbase) : "memory");
}
DEVI void ld4_sc1(unsigned& d, unsigned voff, const void* sbase) {
  asm volatile("global_load_dword %0, %1, %2 sc1" : "=v"(d) : "v"(voff), "s"(sbase) : "memory");
}
DEVI void st16_sc1(v4u d, unsigned voff, void* sbase) {
  asm volatile("global_store_dwordx4 %0, %1, %2 sc1\n\ts_nop 1" :: "v"(voff), "v"(d), "s"(sbase) : "memory");
}
DEVI void st16(v4u d, unsigned voff, void* sbase) {
  asm volatile("global_store_dwordx4 %0, %1, %2\n\ts_nop 1" :: "v"(voff), "v"(d), "s"(sbase) : "memory");
}
DEVI void st8_sc1(v2u d, unsigned voff, void* sbase) {
  asm volatile("global_store_dwordx2 %0, %1, %2 sc1" :: "v"(voff), "v"(d), "s"(sbase) : "memory");
}
#define PIN(x) asm volatile("" : "+v"(x))

DEVI float bflo(unsigned w) { return __builtin_bit_cast(float, w << 16); }
DEVI float bfhi(unsigned w) { return __builtin_bit_cast(float, w & 0xffff0000u); }
DEVI unsigned pkbf(float a, float b) { f32x2 f = {a, b}; return __builtin_bit_cast(unsigned, __builtin_convertvector(f, bf16x2)); }
DEVI unsigned pkh(float a, float b) { f32x2 f = {a, b}; return __builtin_bit_cast(unsigned, __builtin_convertvector(f, f16x2)); }
DEVI float rbf(float a) { return bflo(pkbf(a, a)); }
DEVI int imax(int a, int b) { return a > b ? a : b; }
DEVI int imin(int a, int b) { return a < b ? a : b; }
template <int CTRL> DEVI float dppf(float v) {
  return __builtin_bit_cast(float, __builtin_amdgcn_update_dpp(0, __builtin_bit_cast(int, v), CTRL, 0xf, 0xf, true));
}
DEVI float sum32(float v) {
  v += dppf<0xB1>(v); v += dppf<0x4E>(v); v += dppf<0x141>(v); v += dppf<0x140>(v);
  return v + __builtin_bit_cast(float, __builtin_amdgcn_ds_swizzle(__builtin_bit_cast(int, v), 0x401F));
}
DEVI float relu(float a) { return __builtin_bit_cast(float, imax(__builtin_bit_cast(int, a), 0)); }
DEVI int wave_maxi(int v) {
  v = imax(v, __builtin_amdgcn_update_dpp(0, v, 0x111, 0xf, 0xf, true));
  v = imax(v, __builtin_amdgcn_update_dpp(0, v, 0x112, 0xf, 0xf, true));
  v = imax(v, __builtin_amdgcn_update_dpp(0, v, 0x114, 0xf, 0xf, true));
  v = imax(v, __builtin_amdgcn_update_dpp(0, v, 0x118, 0xf, 0xf, true));
  v = imax(v, __builtin_amdgcn_update_dpp(0, v, 0x142, 0xa, 0xf, true));
  v = imax(v, __builtin_amdgcn_update_dpp(0, v, 0x143, 0xc, 0xf, true));
  return __builtin_amdgcn_readlane(v, 63);
}
DEVI float expacc(float x) {
  const float L = 1.44269504088896341f, Ll = 1.925963033500011e-8f;
  float hi = x * L, lo = __builtin_fmaf(x, L, -hi) + x * Ll;
  float e = __builtin_amdgcn_exp2f(hi);
  return __builtin_fmaf(e, lo * 0.6931471805599453f, e);
}
struct cf { float r, i; };
DEVI cf cmul(cf a, cf b) { cf c; c.r = a.r * b.r - a.i * b.i; c.i = a.r * b.i + a.i * b.r; return c; }
DEVI cf cexpp(float ar2, float ai2, float p) {
  float e = __builtin_amdgcn_exp2f(ar2 * p);
  float r = __builtin_amdgcn_fractf(ai2 * p);
  cf c; c.r = e * __builtin_amdgcn_cosf(r); c.i = e * __builtin_amdgcn_sinf(r); return c;
}
DEVI f32x4 mfma16h(v4u a, v4u b, f32x4 c) {
  return __builtin_amdgcn_mfma_f32_16x16x32_f16(__builtin_bit_cast(f16x8, a), __builtin_bit_cast(f16x8, b), c, 0, 0, 0);
}
DEVI bool allv(bool p) { return __builtin_amdgcn_ballot_w64(p) == ~0ull; }

#define LD128(a) (*(LAS const v4u*)(sm + (a)))
#define LDTR(a) __builtin_bit_cast(v2u, __builtin_amdgcn_ds_read_tr16_b64_v4i16((LAS v4s*)(sm + (a))))
#define ST128(a, v) (*(LAS v4u*)(sm + (a)) = (v))
#define ST64(a, v) (*(LAS v2u*)(sm + (a)) = (v))
#define ST16(a, v) (*(LAS u16*)(sm + (a)) = (u16)(v))
#define LD16(a) ((unsigned)*(LAS const u16*)(sm + (a)))
DEVI v4u trfrag(LAS char* sm, int a, int d) { v2u p = LDTR(a), q = LDTR(a + d); return v4u{p.x, p.y, q.x, q.y}; }
DEVI unsigned lds_rd(LAS char* sm, int a) { return __builtin_amdgcn_readfirstlane(*(LAS volatile unsigned*)(sm + a)); }
DEVI unsigned spin_ge(unsigned vaddr, unsigned n) {
  unsigned cnt, tmp, cur;
  asm volatile("s_mov_b32 %1, 0x400000\n"
               "1:\n\tds_read_b32 %2, %3\n\ts_waitcnt lgkmcnt(0)\n\tv_readfirstlane_b32 %0, %2\n\ts_sub_u32 %1, %1, 1\n\t"
               "s_cbranch_scc1 2f\n\ts_cmp_lt_u32 %0, %4\n\ts_cbranch_scc1 1b\n2:"
               : "=&s"(cur), "=&s"(cnt), "=&v"(tmp) : "v"(vaddr), "s"(__builtin_amdgcn_readfirstlane(n)) : "memory", "scc");
  return __builtin_amdgcn_readfirstlane(cur);
}
DEVI void wait_ge(LAS char* sm, int a, unsigned n) {
  unsigned cnt, tmp, cur;
  asm volatile("s_mov_b32 %1, 0x400000\n"
               "1:\n\tds_read_b32 %2, %3\n\ts_waitcnt lgkmcnt(0)\n\tv_readfirstlane_b32 %0, %2\n\ts_cmp_ge_u32 %0, %4\n\t"
               "s_cbranch_scc1 2f\n\ts_sleep 1\n\ts_sub_u32 %1, %1, 1\n\ts_cbranch_scc0 1b\n2:"
               : "=&s"(cur), "=&s"(cnt), "=&v"(tmp) : "v"((unsigned)(uintptr_t)(sm + a)), "s"(n) : "memory", "scc");
}
DEVI f32x4 lanesum4(unsigned a, unsigned b, f32x4 c) {
  const v4s ones = {0x3f80, 0x3f80, 0x3f80, 0x3f80};
  return __builtin_amdgcn_mfma_f32_4x4x4bf16_1k(ones, __builtin_bit_cast(v4s, (v2u{a, b})), c, 0, 0, 0);
}
DEVI void wait_free(LAS char* sm, unsigned n) {
#pragma unroll 1
  for (unsigned i = 0; i < LSPIN_CAP; ++i) {
    v4u f = *(LAS volatile v4u*)(sm + S_FREE);
    unsigned m = f.x < f.y ? f.x : f.y, m2 = f.z < f.w ? f.z : f.w;
    if (__builtin_amdgcn_readfirstlane(m < m2 ? m : m2) >= n) break;
    __builtin_amdgcn_s_sleep(1);
  }
  MEMF();
}
DEVI void sig_set(LAS char* sm, int a, unsigned v) {
  asm volatile("s_mov_b64 exec, 1\n\tds_write_b32 %0, %1\n\ts_mov_b64 exec, -1" :: "v"((unsigned)(uintptr_t)(sm + a)), "v"(v) : "memory");
}
DEVI void sig_add(LAS char* sm, int a, int lane) {
  asm volatile("s_mov_b64 exec, 1\n\tds_add_u32 %0, %1\n\ts_mov_b64 exec, -1" :: "v"((unsigned)(uintptr_t)(sm + a)), "v"(1u) : "memory");
}

DEVI unsigned add_ret(LAS char* sm, int a) {
  unsigned old;
  asm volatile("s_mov_b64 exec, 1\n\tds_add_rtn_u32 %0, %1, %2\n\ts_mov_b64 exec, -1\n\ts_waitcnt lgkmcnt(0)" : "=&v"(old) : "v"((unsigned)(uintptr_t)(sm + a)), "v"(1u) : "memory");
  return __builtin_amdgcn_readfirstlane(old);
}
DEVI void flag_st(unsigned tag, unsigned off, void* base) {
  asm volatile("s_mov_b64 exec, 1\n\tglobal_store_dword %0, %1, %2 sc1\n\ts_mov_b64 exec, -1" :: "v"(off), "v"(tag), "s"(base) : "memory");
}

#define G1_ASM \
  "v_mov_b64 v[64:65], 0\n\t" \
  "v_mov_b64 v[66:67], 0\n\t" \
  "v_mov_b64 v[68:69], 0\n\t" \
  "v_mov_b64 v[70:71], 0\n\t" \
  "v_mov_b64 v[72:73], 0\n\t" \
  "v_mov_b64 v[74:75], 0\n\t" \
  "v_mov_b64 v[76:77], 0\n\t" \
  "v_mov_b64 v[78:79], 0\n\t" \
  "v_mov_b64 v[80:81], 0\n\t" \
  "v_mov_b64 v[82:83], 0\n\t" \
  "v_mov_b64 v[84:85], 0\n\t" \
  "v_mov_b64 v[86:87], 0\n\t" \
  "v_mov_b64 v[88:89], 0\n\t" \
  "v_mov_b64 v[90:91], 0\n\t" \
  "v_mov_b64 v[92:93], 0\n\t" \
  "v_mov_b64 v[94:95], 0\n\t" \
  "v_mov_b64 v[96:97], 0\n\t" \
  "v_mov_b64 v[98:99], 0\n\t" \
  "v_mov_b64 v[100:101], 0\n\t" \
  "v_mov_b64 v[102:103], 0\n\t" \
  "v_mov_b64 v[104:105], 0\n\t" \
  "v_mov_b64 v[106:107], 0\n\t" \
  "v_mov_b64 v[108:109], 0\n\t" \
  "v_mov_b64 v[110:111], 0\n\t" \
  "v_mov_b64 v[112:113], 0\n\t" \
  "v_mov_b64 v[114:115], 0\n\t" \
  "v_mov_b64 v[116:117], 0\n\t" \
  "v_mov_b64 v[118:119], 0\n\t" \
  "v_mov_b64 v[120:121], 0\n\t" \
  "v_mov_b64 v[122:123], 0\n\t" \
  "v_mov_b64 v[124:125], 0\n\t" \
  "v_mov_b64 v[126:127], 0\n\t" \
  "v_mov_b64 v[128:129], 0\n\t" \
  "v_mov_b64 v[130:131], 0\n\t" \
  "v_mov_b64 v[132:133], 0\n\t" \
  "v_mov_b64 v[134:135], 0\n\t" \
  "v_mov_b64 v[136:137], 0\n\t" \
  "v_mov_b64 v[138:139], 0\n\t" \
  "v_mov_b64 v[140:141], 0\n\t" \
  "v_mov_b64 v[142:143], 0\n\t" \
  "v_mov_b64 v[144:145], 0\n\t" \
  "v_mov_b64 v[146:147], 0\n\t" \
  "v_mov_b64 v[148:149], 0\n\t" \
  "v_mov_b64 v[150:151], 0\n\t" \
  "v_mov_b64 v[152:153], 0\n\t" \
  "v_mov_b64 v[154:155], 0\n\t" \
  "v_mov_b64 v[156:157], 0\n\t" \
  "v_mov_b64 v[158:159], 0\n\t" \
  "s_mov_b32 s90, 0x400000\n\t" \
  "10:\n\t" \
  "ds_read_b32 v240, %15\n\t" \
  "s_waitcnt lgkmcnt(0)\n\t" \
  "v_readfirstlane_b32 %12, v240\n\t" \
  "s_sub_u32 s90, s90, 1\n\t" \
  "s_cbranch_scc1 11f\n\t" \
  "s_cmp_lt_u32 %12, 2\n\t" \
  "s_cbranch_scc1 10b\n\t" \
  "11:\n\t" \
  "ds_read_b128 v[160:163], %0\n\t" \
  "ds_read_b64_tr_b16 v[168:169], %4 offset:0\n\t" \
  "ds_read_b64_tr_b16 v[170:171], %4 offset:768\n\t" \
  "ds_read_b64_tr_b16 v[172:173], %5 offset:0\n\t" \
  "ds_read_b64_tr_b16 v[174:175], %5 offset:768\n\t" \
  "ds_read_b64_tr_b16 v[176:177], %6 offset:0\n\t" \
  "ds_read_b64_tr_b16 v[178:179], %6 offset:768\n\t" \
  "ds_read_b128 v[164:167], %0 offset:4096\n\t" \
  "s_waitcnt lgkmcnt(4)\n\t" \
  "ds_read_b128 v[180:183], %1\n\t" \
  "ds_read_b64_tr_b16 v[188:189], %4 offset:6144\n\t" \
  "ds_read_b64_tr_b16 v[190:191], %4 offset:6912\n\t" \
  "ds_read_b64_tr_b16 v[192:193], %5 offset:6144\n\t" \
  "ds_read_b64_tr_b16 v[194:195], %5 offset:6912\n\t" \
  "ds_read_b64_tr_b16 v[196:197], %6 offset:6144\n\t" \
  "ds_read_b64_tr_b16 v[198:199], %6 offset:6912\n\t" \
  "ds_read_b128 v[184:187], %1 offset:4096\n\t" \
  "v_add_u32 %0, %9, %0\n\t" \
  "v_add_u32 %1, %9, %1\n\t" \
  "s_waitcnt lgkmcnt(0)\n\t" \
  "20:\n\t" \
  "v_mfma_f32_32x32x16_bf16 v[64:79], v[168:171], v[160:163], v[64:79]\n\t" \
  "s_waitcnt lgkmcnt(5)\n\t" \
  "v_readfirstlane_b32 %12, v240\n\t" \
  "v_mfma_f32_32x32x16_bf16 v[80:95], v[172:175], v[160:163], v[80:95]\n\t" \
  "s_cmp_lt_u32 %12, %8\n\t" \
  "s_cbranch_scc1 30f\n\t" \
  "21:\n\t" \
  "ds_read_b128 v[200:203], %2\n\t" \
  "v_mfma_f32_32x32x16_bf16 v[96:111], v[176:179], v[160:163], v[96:111]\n\t" \
  "ds_read_b64_tr_b16 v[208:209], %4 offset:12288\n\t" \
  "ds_read_b64_tr_b16 v[210:211], %4 offset:13056\n\t" \
  "s_add_u32 %8, %8, 1\n\t" \
  "v_mfma_f32_32x32x16_bf16 v[112:127], v[168:171], v[164:167], v[112:127]\n\t" \
  "ds_read_b32 v240, %15\n\t" \
  "ds_read_b64_tr_b16 v[212:213], %5 offset:12288\n\t" \
  "ds_read_b64_tr_b16 v[214:215], %5 offset:13056\n\t" \
  "v_mfma_f32_32x32x16_bf16 v[128:143], v[172:175], v[164:167], v[128:143]\n\t" \
  "ds_read_b64_tr_b16 v[216:217], %6 offset:12288\n\t" \
  "ds_read_b64_tr_b16 v[218:219], %6 offset:13056\n\t" \
  "v_add_u32 %7, 1, %7\n\t" \
  "v_mfma_f32_32x32x16_bf16 v[144:159], v[176:179], v[164:167], v[144:159]\n\t" \
  "ds_read_b128 v[204:207], %2 offset:4096\n\t" \
  "v_add_u32 %2, %9, %2\n\t" \
  "v_mfma_f32_32x32x16_bf16 v[64:79], v[188:191], v[180:183], v[64:79]\n\t" \
  "s_waitcnt lgkmcnt(5)\n\t" \
  "v_readfirstlane_b32 %12, v240\n\t" \
  "v_mfma_f32_32x32x16_bf16 v[80:95], v[192:195], v[180:183], v[80:95]\n\t" \
  "s_cmp_lt_u32 %12, %8\n\t" \
  "s_cbranch_scc1 31f\n\t" \
  "22:\n\t" \
  "ds_read_b128 v[220:223], %3\n\t" \
  "v_mfma_f32_32x32x16_bf16 v[96:111], v[196:199], v[180:183], v[96:111]\n\t" \
  "ds_read_b64_tr_b16 v[228:229], %4 offset:18432\n\t" \
  "ds_read_b64_tr_b16 v[230:231], %4 offset:19200\n\t" \
  "s_add_u32 %8, %8, 1\n\t" \
  "v_mfma_f32_32x32x16_bf16 v[112:127], v[188:191], v[184:187], v[112:127]\n\t" \
  "ds_read_b32 v240, %15\n\t" \
  "ds_read_b64_tr_b16 v[232:233], %5 offset:18432\n\t" \
  "ds_read_b64_tr_b16 v[234:235], %5 offset:19200\n\t" \
  "v_add_u32 %4, %9, %4\n\t" \
  "v_mfma_f32_32x32x16_bf16 v[128:143], v[192:195], v[184:187], v[128:143]\n\t" \
  "ds_read_b64_tr_b16 v[236:237], %6 offset:18432\n\t" \
  "ds_read_b64_tr_b16 v[238:239], %6 offset:19200\n\t" \
  "v_add_u32 %5, %9, %5\n\t" \
  "v_mfma_f32_32x32x16_bf16 v[144:159], v[196:199], v[184:187], v[144:159]\n\t" \
  "ds_read_b128 v[224:227], %3 offset:4096\n\t" \
  "s_mov_b64 exec, 1\n\t" \
  "ds_write_b32 %16, %7\n\t" \
  "s_mov_b64 exec, -1\n\t" \
  "v_add_u32 %6, %9, %6\n\t" \
  "v_mfma_f32_32x32x16_bf16 v[64:79], v[208:211], v[200:203], v[64:79]\n\t" \
  "s_waitcnt lgkmcnt(6)\n\t" \
  "v_readfirstlane_b32 %12, v240\n\t" \
  "v_mfma_f32_32x32x16_bf16 v[80:95], v[212:215], v[200:203], v[80:95]\n\t" \
  "s_cmp_lt_u32 %12, %8\n\t" \
  "s_cbranch_scc1 32f\n\t" \
  "23:\n\t" \
  "ds_read_b128 v[160:163], %0\n\t" \
  "v_mfma_f32_32x32x16_bf16 v[96:111], v[216:219], v[200:203], v[96:111]\n\t" \
  "ds_read_b64_tr_b16 v[168:169], %4 offset:0\n\t" \
  "ds_read_b64_tr_b16 v[170:171], %4 offset:768\n\t" \
  "s_add_u32 %8, %8, 1\n\t" \
  "v_mfma_f32_32x32x16_bf16 v[112:127], v[208:211], v[204:207], v[112:127]\n\t" \
  "ds_read_b32 v240, %15\n\t" \
  "ds_read_b64_tr_b16 v[172:173], %5 offset:0\n\t" \
  "ds_read_b64_tr_b16 v[174:175], %5 offset:768\n\t" \
  "v_mfma_f32_32x32x16_bf16 v[128:143], v[212:215], v[204:207], v[128:143]\n\t" \
  "ds_read_b64_tr_b16 v[176:177], %6 offset:0\n\t" \
  "ds_read_b64_tr_b16 v[178:179], %6 offset:768\n\t" \
  "v_mfma_f32_32x32x16_bf16 v[144:159], v[216:219], v[204:207], v[144:159]\n\t" \
  "ds_read_b128 v[164:167], %0 offset:4096\n\t" \
  "v_add_u32 %3, %9, %3\n\t" \
  "v_add_u32 %0, %10, %0\n\t" \
  "v_mfma_f32_32x32x16_bf16 v[64:79], v[228:231], v[220:223], v[64:79]\n\t" \
  "s_waitcnt lgkmcnt(5)\n\t" \
  "v_readfirstlane_b32 %12, v240\n\t" \
  "v_mfma_f32_32x32x16_bf16 v[80:95], v[232:235], v[220:223], v[80:95]\n\t" \
  "s_cmp_lt_u32 %12, %8\n\t" \
  "s_cbranch_scc1 33f\n\t" \
  "24:\n\t" \
  "ds_read_b128 v[180:183], %1\n\t" \
  "v_mfma_f32_32x32x16_bf16 v[96:111], v[236:239], v[220:223], v[96:111]\n\t" \
  "ds_read_b64_tr_b16 v[188:189], %4 offset:6144\n\t" \
  "ds_read_b64_tr_b16 v[190:191], %4 offset:6912\n\t" \
  "s_add_u32 %8, %8, 1\n\t" \
  "v_mfma_f32_32x32x16_bf16 v[112:127], v[228:231], v[224:227], v[112:127]\n\t" \
  "ds_read_b32 v240, %15\n\t" \
  "ds_read_b64_tr_b16 v[192:193], %5 offset:6144\n\t" \
  "ds_read_b64_tr_b16 v[194:195], %5 offset:6912\n\t" \
  "v_mfma_f32_32x32x16_bf16 v[128:143], v[232:235], v[224:227], v[128:143]\n\t" \
  "ds_read_b64_tr_b16 v[196:197], %6 offset:6144\n\t" \
  "ds_read_b64_tr_b16 v[198:199], %6 offset:6912\n\t" \
  "s_mov_b32 %13, %9\n\t" \
  "v_mfma_f32_32x32x16_bf16 v[144:159], v[236:239], v[224:227], v[144:159]\n\t" \
  "ds_read_b128 v[184:187], %1 offset:4096\n\t" \
  "v_add_u32 %1, %10, %1\n\t" \
  "s_mov_b32 %9, %10\n\t" \
  "s_mov_b32 %10, %11\n\t" \
  "s_mov_b32 %11, %13\n\t" \
  "s_sub_u32 %14, %14, 1\n\t" \
  "s_cbranch_scc0 20b\n\t" \
  "v_mfma_f32_32x32x16_bf16 v[64:79], v[168:171], v[160:163], v[64:79]\n\t" \
  "s_waitcnt lgkmcnt(5)\n\t" \
  "v_add_u32 %7, 1, %7\n\t" \
  "v_mfma_f32_32x32x16_bf16 v[80:95], v[172:175], v[160:163], v[80:95]\n\t" \
  "v_mfma_f32_32x32x16_bf16 v[96:111], v[176:179], v[160:163], v[96:111]\n\t" \
  "v_mfma_f32_32x32x16_bf16 v[112:127], v[168:171], v[164:167], v[112:127]\n\t" \
  "v_mfma_f32_32x32x16_bf16 v[128:143], v[172:175], v[164:167], v[128:143]\n\t" \
  "v_mfma_f32_32x32x16_bf16 v[144:159], v[176:179], v[164:167], v[144:159]\n\t" \
  "v_mfma_f32_32x32x16_bf16 v[64:79], v[188:191], v[180:183], v[64:79]\n\t" \
  "s_mov_b32 s90, 0x400000\n\t" \
  "60:\n\t" \
  "ds_read_b32 v240, %15\n\t" \
  "s_waitcnt lgkmcnt(0)\n\t" \
  "v_readfirstlane_b32 %12, v240\n\t" \
  "s_sub_u32 s90, s90, 1\n\t" \
  "s_cbranch_scc1 61f\n\t" \
  "s_cmp_lt_u32 %12, 31\n\t" \
  "s_cbranch_scc1 60b\n\t" \
  "61:\n\t" \
  "ds_read_b128 v[200:203], %2\n\t" \
  "ds_read_b64_tr_b16 v[208:209], %4 offset:12288\n\t" \
  "ds_read_b64_tr_b16 v[210:211], %4 offset:13056\n\t" \
  "v_mfma_f32_32x32x16_bf16 v[80:95], v[192:195], v[180:183], v[80:95]\n\t" \
  "ds_read_b64_tr_b16 v[212:213], %5 offset:12288\n\t" \
  "ds_read_b64_tr_b16 v[214:215], %5 offset:13056\n\t" \
  "ds_read_b64_tr_b16 v[216:217], %6 offset:12288\n\t" \
  "ds_read_b64_tr_b16 v[218:219], %6 offset:13056\n\t" \
  "v_mfma_f32_32x32x16_bf16 v[96:111], v[196:199], v[180:183], v[96:111]\n\t" \
  "ds_read_b128 v[204:207], %2 offset:4096\n\t" \
  "v_mov_b64 v[160:161], 0\n\t" \
  "v_mov_b64 v[162:163], 0\n\t" \
  "v_mov_b64 v[164:165], 0\n\t" \
  "v_mov_b64 v[166:167], 0\n\t" \
  "v_mov_b64 v[170:171], 0\n\t" \
  "v_mov_b64 v[174:175], 0\n\t" \
  "v_mov_b64 v[178:179], 0\n\t" \
  "v_mfma_f32_32x32x16_bf16 v[112:127], v[188:191], v[184:187], v[112:127]\n\t" \
  "s_mov_b32 s90, 0x400000\n\t" \
  "62:\n\t" \
  "ds_read_b32 v240, %17\n\t" \
  "s_waitcnt lgkmcnt(0)\n\t" \
  "v_readfirstlane_b32 %12, v240\n\t" \
  "s_sub_u32 s90, s90, 1\n\t" \
  "s_cbranch_scc1 63f\n\t" \
  "s_cmp_lt_u32 %12, 1\n\t" \
  "s_cbranch_scc1 62b\n\t" \
  "63:\n\t" \
  "ds_read_b64 v[168:169], %18 offset:0\n\t" \
  "ds_read_b64 v[172:173], %18 offset:256\n\t" \
  "ds_read_b64 v[176:177], %18 offset:512\n\t" \
  "s_mov_b32 exec_hi, 0\n\t" \
  "ds_read_b64 v[160:161], %19 offset:0\n\t" \
  "ds_read_b64 v[164:165], %19 offset:256\n\t" \
  "s_mov_b32 exec_hi, -1\n\t" \
  "v_mfma_f32_32x32x16_bf16 v[128:143], v[192:195], v[184:187], v[128:143]\n\t" \
  "v_mfma_f32_32x32x16_bf16 v[144:159], v[196:199], v[184:187], v[144:159]\n\t" \
  "v_mfma_f32_32x32x16_bf16 v[64:79], v[208:211], v[200:203], v[64:79]\n\t" \
  "v_mfma_f32_32x32x16_bf16 v[80:95], v[212:215], v[200:203], v[80:95]\n\t" \
  "v_mfma_f32_32x32x16_bf16 v[96:111], v[216:219], v[200:203], v[96:111]\n\t" \
  "v_mfma_f32_32x32x16_bf16 v[112:127], v[208:211], v[204:207], v[112:127]\n\t" \
  "v_mfma_f32_32x32x16_bf16 v[128:143], v[212:215], v[204:207], v[128:143]\n\t" \
  "v_mfma_f32_32x32x16_bf16 v[144:159], v[216:219], v[204:207], v[144:159]\n\t" \
  "s_waitcnt lgkmcnt(0)\n\t" \
  "v_mfma_f32_32x32x16_bf16 v[64:79], v[168:171], v[160:163], v[64:79]\n\t" \
  "v_mfma_f32_32x32x16_bf16 v[80:95], v[172:175], v[160:163], v[80:95]\n\t" \
  "v_mfma_f32_32x32x16_bf16 v[96:111], v[176:179], v[160:163], v[96:111]\n\t" \
  "s_mov_b32 s90, 0x400000\n\t" \
  "64:\n\t" \
  "ds_read_b32 v240, %15\n\t" \
  "s_waitcnt lgkmcnt(0)\n\t" \
  "v_readfirstlane_b32 %12, v240\n\t" \
  "s_sub_u32 s90, s90, 1\n\t" \
  "s_cbranch_scc1 65f\n\t" \
  "s_cmp_lt_u32 %12, 32\n\t" \
  "s_cbranch_scc1 64b\n\t" \
  "65:\n\t" \
  "ds_read_b128 v[220:223], %3\n\t" \
  "ds_read_b64_tr_b16 v[228:229], %4 offset:18432\n\t" \
  "ds_read_b64_tr_b16 v[230:231], %4 offset:19200\n\t" \
  "ds_read_b64_tr_b16 v[232:233], %5 offset:18432\n\t" \
  "ds_read_b64_tr_b16 v[234:235], %5 offset:19200\n\t" \
  "v_mfma_f32_32x32x16_bf16 v[112:127], v[168:171], v[164:167], v[112:127]\n\t" \
  "ds_read_b64_tr_b16 v[236:237], %6 offset:18432\n\t" \
  "ds_read_b64_tr_b16 v[238:239], %6 offset:19200\n\t" \
  "ds_read_b128 v[224:227], %3 offset:4096\n\t" \
  "s_mov_b64 exec, 1\n\t" \
  "ds_write_b32 %16, %7\n\t" \
  "s_mov_b64 exec, -1\n\t" \
  "v_mfma_f32_32x32x16_bf16 v[128:143], v[172:175], v[164:167], v[128:143]\n\t" \
  "v_mfma_f32_32x32x16_bf16 v[144:159], v[176:179], v[164:167], v[144:159]\n\t" \
  "s_setprio 3\n\t" \
  "s_cmp_eq_u32 %23, 14\n\t" \
  "s_cselect_b64 vcc, -1, 0\n\t" \
  "s_waitcnt lgkmcnt(6)\n\t" \
  "v_mfma_f32_32x32x16_bf16 v[64:79], v[228:231], v[220:223], v[64:79]\n\t" \
  "s_waitcnt lgkmcnt(4)\n\t" \
  "v_mfma_f32_32x32x16_bf16 v[80:95], v[232:235], v[220:223], v[80:95]\n\t" \
  "s_waitcnt lgkmcnt(2)\n\t" \
  "v_mfma_f32_32x32x16_bf16 v[96:111], v[236:239], v[220:223], v[96:111]\n\t" \
  "s_waitcnt lgkmcnt(0)\n\t" \
  "s_nop 7\n\t" \
  "v_max_i32 v64, 0, v64\n\t" \
  "v_max_i32 v65, 0, v65\n\t" \
  "v_max_i32 v66, 0, v66\n\t" \
  "v_max_i32 v67, 0, v67\n\t" \
  "v_max_i32 v68, 0, v68\n\t" \
  "v_max_i32 v69, 0, v69\n\t" \
  "v_mfma_f32_32x32x16_bf16 v[112:127], v[228:231], v[224:227], v[112:127]\n\t" \
  "v_max_i32 v70, 0, v70\n\t" \
  "v_max_i32 v71, 0, v71\n\t" \
  "v_mul_f32 v64, v64, v72\n\t" \
  "v_mul_f32 v65, v65, v73\n\t" \
  "v_mul_f32 v66, v66, v74\n\t" \
  "v_mul_f32 v67, v67, v75\n\t" \
  "v_mfma_f32_32x32x16_bf16 v[128:143], v[232:235], v[224:227], v[128:143]\n\t" \
  "v_mul_f32 v68, v68, v76\n\t" \
  "v_mul_f32 v69, v69, v77\n\t" \
  "v_mul_f32 v70, v70, v78\n\t" \
  "v_mul_f32 v71, v71, v79\n\t" \
  "v_cvt_pk_bf16_f32 v64, v64, v65\n\t" \
  "v_cvt_pk_bf16_f32 v65, v66, v67\n\t" \
  "v_mfma_f32_32x32x16_bf16 v[144:159], v[236:239], v[224:227], v[144:159]\n\t" \
  "v_cvt_pk_bf16_f32 v66, v68, v69\n\t" \
  "v_cvt_pk_bf16_f32 v67, v70, v71\n\t" \
  "s_cbranch_vccnz 70f\n\t" \
  "global_store_dwordx4 %20, v[64:67], %21 offset:0 sc1\n\t" \
  "70:\n\t" \
  "v_max_i32 v80, 0, v80\n\t" \
  "v_max_i32 v81, 0, v81\n\t" \
  "v_max_i32 v82, 0, v82\n\t" \
  "v_max_i32 v83, 0, v83\n\t" \
  "v_max_i32 v84, 0, v84\n\t" \
  "v_max_i32 v85, 0, v85\n\t" \
  "v_max_i32 v86, 0, v86\n\t" \
  "v_max_i32 v87, 0, v87\n\t" \
  "v_mul_f32 v80, v80, v88\n\t" \
  "v_mul_f32 v81, v81, v89\n\t" \
  "v_mul_f32 v82, v82, v90\n\t" \
  "v_mul_f32 v83, v83, v91\n\t" \
  "v_mul_f32 v84, v84, v92\n\t" \
  "v_mul_f32 v85, v85, v93\n\t" \
  "v_mul_f32 v86, v86, v94\n\t" \
  "v_mul_f32 v87, v87, v95\n\t" \
  "v_cvt_pk_bf16_f32 v80, v80, v81\n\t" \
  "v_cvt_pk_bf16_f32 v81, v82, v83\n\t" \
  "v_cvt_pk_bf16_f32 v82, v84, v85\n\t" \
  "v_cvt_pk_bf16_f32 v83, v86, v87\n\t" \
  "global_store_dwordx4 %20, v[80:83], %21 offset:1024 sc1\n\t" \
  "v_max_i32 v96, 0, v96\n\t" \
  "v_max_i32 v97, 0, v97\n\t" \
  "v_max_i32 v98, 0, v98\n\t" \
  "v_max_i32 v99, 0, v99\n\t" \
  "v_max_i32 v100, 0, v100\n\t" \
  "v_max_i32 v101, 0, v101\n\t" \
  "v_max_i32 v102, 0, v102\n\t" \
  "v_max_i32 v103, 0, v103\n\t" \
  "v_mul_f32 v96, v96, v104\n\t" \
  "v_mul_f32 v97, v97, v105\n\t" \
  "v_mul_f32 v98, v98, v106\n\t" \
  "v_mul_f32 v99, v99, v107\n\t" \
  "v_mul_f32 v100, v100, v108\n\t" \
  "v_mul_f32 v101, v101, v109\n\t" \
  "v_mul_f32 v102, v102, v110\n\t" \
  "v_mul_f32 v103, v103, v111\n\t" \
  "v_cvt_pk_bf16_f32 v96, v96, v97\n\t" \
  "v_cvt_pk_bf16_f32 v97, v98, v99\n\t" \
  "v_cvt_pk_bf16_f32 v98, v100, v101\n\t" \
  "v_cvt_pk_bf16_f32 v99, v102, v103\n\t" \
  "global_store_dwordx4 %20, v[96:99], %21 offset:2048 sc1\n\t" \
  "v_max_i32 v112, 0, v112\n\t" \
  "v_max_i32 v113, 0, v113\n\t" \
  "v_max_i32 v114, 0, v114\n\t" \
  "v_max_i32 v115, 0, v115\n\t" \
  "v_max_i32 v116, 0, v116\n\t" \
  "v_max_i32 v117, 0, v117\n\t" \
  "v_max_i32 v118, 0, v118\n\t" \
  "v_max_i32 v119, 0, v119\n\t" \
  "v_mul_f32 v112, v112, v120\n\t" \
  "v_mul_f32 v113, v113, v121\n\t" \
  "v_mul_f32 v114, v114, v122\n\t" \
  "v_mul_f32 v115, v115, v123\n\t" \
  "v_mul_f32 v116, v116, v124\n\t" \
  "v_mul_f32 v117, v117, v125\n\t" \
  "v_mul_f32 v118, v118, v126\n\t" \
  "v_mul_f32 v119, v119, v127\n\t" \
  "v_cvt_pk_bf16_f32 v112, v112, v113\n\t" \
  "v_cvt_pk_bf16_f32 v113, v114, v115\n\t" \
  "v_cvt_pk_bf16_f32 v114, v116, v117\n\t" \
  "v_cvt_pk_bf16_f32 v115, v118, v119\n\t" \
  "s_cbranch_vccnz 71f\n\t" \
  "global_store_dwordx4 %20, v[112:115], %22 offset:0 sc1\n\t" \
  "71:\n\t" \
  "v_max_i32 v128, 0, v128\n\t" \
  "v_max_i32 v129, 0, v129\n\t" \
  "v_max_i32 v130, 0, v130\n\t" \
  "v_max_i32 v131, 0, v131\n\t" \
  "v_max_i32 v132, 0, v132\n\t" \
  "v_max_i32 v133, 0, v133\n\t" \
  "v_max_i32 v134, 0, v134\n\t" \
  "v_max_i32 v135, 0, v135\n\t" \
  "v_mul_f32 v128, v128, v136\n\t" \
  "v_mul_f32 v129, v129, v137\n\t" \
  "v_mul_f32 v130, v130, v138\n\t" \
  "v_mul_f32 v131, v131, v139\n\t" \
  "v_mul_f32 v132, v132, v140\n\t" \
  "v_mul_f32 v133, v133, v141\n\t" \
  "v_mul_f32 v134, v134, v142\n\t" \
  "v_mul_f32 v135, v135, v143\n\t" \
  "v_cvt_pk_bf16_f32 v128, v128, v129\n\t" \
  "v_cvt_pk_bf16_f32 v129, v130, v131\n\t" \
  "v_cvt_pk_bf16_f32 v130, v132, v133\n\t" \
  "v_cvt_pk_bf16_f32 v131, v134, v135\n\t" \
  "global_store_dwordx4 %20, v[128:131], %22 offset:1024 sc1\n\t" \
  "v_max_i32 v144, 0, v144\n\t" \
  "v_max_i32 v145, 0, v145\n\t" \
  "v_max_i32 v146, 0, v146\n\t" \
  "v_max_i32 v147, 0, v147\n\t" \
  "v_max_i32 v148, 0, v148\n\t" \
  "v_max_i32 v149, 0, v149\n\t" \
  "v_max_i32 v150, 0, v150\n\t" \
  "v_max_i32 v151, 0, v151\n\t" \
  "v_mul_f32 v144, v144, v152\n\t" \
  "v_mul_f32 v145, v145, v153\n\t" \
  "v_mul_f32 v146, v146, v154\n\t" \
  "v_mul_f32 v147, v147, v155\n\t" \
  "v_mul_f32 v148, v148, v156\n\t" \
  "v_mul_f32 v149, v149, v157\n\t" \
  "v_mul_f32 v150, v150, v158\n\t" \
  "v_mul_f32 v151, v151, v159\n\t" \
  "v_cvt_pk_bf16_f32 v144, v144, v145\n\t" \
  "v_cvt_pk_bf16_f32 v145, v146, v147\n\t" \
  "v_cvt_pk_bf16_f32 v146, v148, v149\n\t" \
  "v_cvt_pk_bf16_f32 v147, v150, v151\n\t" \
  "global_store_dwordx4 %20, v[144:147], %22 offset:2048 sc1\n\t" \
  "s_branch 40f\n\t" \
  "30:\n\t" \
  "s_mov_b32 s90, 0x400000\n\t" \
  "1:\n\t" \
  "ds_read_b32 v240, %15\n\t" \
  "s_waitcnt lgkmcnt(0)\n\t" \
  "v_readfirstlane_b32 %12, v240\n\t" \
  "s_sub_u32 s90, s90, 1\n\t" \
  "s_cbranch_scc1 21b\n\t" \
  "s_cmp_lt_u32 %12, %8\n\t" \
  "s_cbranch_scc1 1b\n\t" \
  "s_branch 21b\n\t" \
  "31:\n\t" \
  "s_mov_b32 s90, 0x400000\n\t" \
  "1:\n\t" \
  "ds_read_b32 v240, %15\n\t" \
  "s_waitcnt lgkmcnt(0)\n\t" \
  "v_readfirstlane_b32 %12, v240\n\t" \
  "s_sub_u32 s90, s90, 1\n\t" \
  "s_cbranch_scc1 22b\n\t" \
  "s_cmp_lt_u32 %12, %8\n\t" \
  "s_cbranch_scc1 1b\n\t" \
  "s_branch 22b\n\t" \
  "32:\n\t" \
  "s_mov_b32 s90, 0x400000\n\t" \
  "1:\n\t" \
  "ds_read_b32 v240, %15\n\t" \
  "s_waitcnt lgkmcnt(0)\n\t" \
  "v_readfirstlane_b32 %12, v240\n\t" \
  "s_sub_u32 s90, s90, 1\n\t" \
  "s_cbranch_scc1 23b\n\t" \
  "s_cmp_lt_u32 %12, %8\n\t" \
  "s_cbranch_scc1 1b\n\t" \
  "s_branch 23b\n\t" \
  "33:\n\t" \
  "s_mov_b32 s90, 0x400000\n\t" \
  "1:\n\t" \
  "ds_read_b32 v240, %15\n\t" \
  "s_waitcnt lgkmcnt(0)\n\t" \
  "v_readfirstlane_b32 %12, v240\n\t" \
  "s_sub_u32 s90, s90, 1\n\t" \
  "s_cbranch_scc1 24b\n\t" \
  "s_cmp_lt_u32 %12, %8\n\t" \
  "s_cbranch_scc1 1b\n\t" \
  "s_branch 24b\n\t" \
  "40:\n\t" \
  "s_nop 1\n\t" \
  ""
#define G1_CLOB "v64", "v65", "v66", "v67", "v68", "v69", "v70", "v71", "v72", "v73", "v74", "v75", "v76", "v77", "v78", "v79", "v80", "v81", "v82", "v83", "v84", "v85", "v86", "v87", "v88", "v89", "v90", "v91", "v92", "v93", "v94", "v95", "v96", "v97", "v98", "v99", "v100", "v101", "v102", "v103", "v104", "v105", "v106", "v107", "v108", "v109", "v110", "v111", "v112", "v113", "v114", "v115", "v116", "v117", "v118", "v119", "v120", "v121", "v122", "v123", "v124", "v125", "v126", "v127", "v128", "v129", "v130", "v131", "v132", "v133", "v134", "v135", "v136", "v137", "v138", "v139", "v140", "v141", "v142", "v143", "v144", "v145", "v146", "v147", "v148", "v149", "v150", "v151", "v152", "v153", "v154", "v155", "v156", "v157", "v158", "v159", "v160", "v161", "v162", "v163", "v164", "v165", "v166", "v167", "v168", "v169", "v170", "v171", "v172", "v173", "v174", "v175", "v176", "v177", "v178", "v179", "v180", "v181", "v182", "v183", "v184", "v185", "v186", "v187", "v188", "v189", "v190", "v191", "v192", "v193", "v194", "v195", "v196", "v197", "v198", "v199", "v200", "v201", "v202", "v203", "v204", "v205", "v206", "v207", "v208", "v209", "v210", "v211", "v212", "v213", "v214", "v215", "v216", "v217", "v218", "v219", "v220", "v221", "v222", "v223", "v224", "v225", "v226", "v227", "v228", "v229", "v230", "v231", "v232", "v233", "v234", "v235", "v236", "v237", "v238", "v239", "v240", "s90"
#define G2_ASM \
  "s_mov_b32 s92, 0\n\t" \
  "s_mov_b32 s93, 0x400000\n\t" \
  "70:\n\t" \
  "ds_read_b128 v[228:231], %5 offset:16\n\t" \
  "s_waitcnt lgkmcnt(0)\n\t" \
  "v_min3_u32 v228, v228, v229, v230\n\t" \
  "v_min_u32 v228, v228, v231\n\t" \
  "s_sub_u32 s93, s93, 1\n\t" \
  "v_readfirstlane_b32 s84, v228\n\t" \
  "s_cbranch_scc1 71f\n\t" \
  "s_cmp_ge_u32 s84, 8\n\t" \
  "s_cbranch_scc1 71f\n\t" \
  "s_sleep 1\n\t" \
  "s_branch 70b\n\t" \
  "71:\n\t" \
  "v_add_u32 v233, 0x1c00, %14\n\t" \
  "s_mul_i32 s84, %9, 0xffff4000\n\t" \
  "s_sub_u32 s84, s84, 0xc000\n\t" \
  "s_mov_b64 s[80:81], %15\n\t" \
  "s_add_u32 s80, s80, s84\n\t" \
  "s_addc_u32 s81, s81, -1\n\t" \
  "s_add_u32 s82, s80, 0xab800\n\t" \
  "s_addc_u32 s83, s81, 0\n\t" \
  "s_mov_b32 s84, %18\n\t" \
  "s_mov_b32 m0, s84\n\t" \
  "s_add_u32 s84, s84, 0x1000\n\t" \
  "global_load_lds_dwordx4 %14, s[80:81]\n\t" \
  "global_load_lds_dwordx4 v233, s[80:81] offset:1024\n\t" \
  "global_load_lds_dwordx4 %14, s[82:83] offset:2048\n\t" \
  "global_load_lds_dwordx4 v233, s[82:83] offset:3072\n\t" \
  "s_add_u32 s80, s80, 0x4000\n\t" \
  "s_addc_u32 s81, s81, 0\n\t" \
  "s_add_u32 s82, s82, 0x4000\n\t" \
  "s_addc_u32 s83, s83, 0\n\t" \
  "s_mov_b32 m0, s84\n\t" \
  "s_add_u32 s84, s84, 0x1000\n\t" \
  "global_load_lds_dwordx4 %14, s[80:81]\n\t" \
  "global_load_lds_dwordx4 v233, s[80:81] offset:1024\n\t" \
  "global_load_lds_dwordx4 %14, s[82:83] offset:2048\n\t" \
  "global_load_lds_dwordx4 v233, s[82:83] offset:3072\n\t" \
  "ds_read_b32 v232, %5\n\t" \
  "s_mov_b32 s87, 0\n\t" \
  "s_waitcnt lgkmcnt(0)\n\t" \
  "v_readfirstlane_b32 s86, v232\n\t" \
  "s_cmp_ge_u32 s86, 46\n\t" \
  "s_cbranch_scc1 81f\n\t" \
  "s_call_b64 s[88:89], 80f\n\t" \
  "s_mov_b32 s87, 1\n\t" \
  "s_mov_b32 s93, 0x400000\n\t" \
  "100:\n\t" \
  "ds_read_b32 v232, %5 offset:0\n\t" \
  "s_waitcnt lgkmcnt(0)\n\t" \
  "v_readfirstlane_b32 s86, v232\n\t" \
  "s_sub_u32 s93, s93, 1\n\t" \
  "s_cbranch_scc1 101f\n\t" \
  "s_cmp_ge_u32 s86, 46\n\t" \
  "s_cbranch_scc1 101f\n\t" \
  "s_sleep 1\n\t" \
  "s_branch 100b\n\t" \
  "101:\n\t" \
  "81:\n\t" \
  "ds_read_b64_tr_b16 v[64:65], %2 offset:0\n\t" \
  "ds_read_b64_tr_b16 v[66:67], %2 offset:1024\n\t" \
  "ds_read_b64_tr_b16 v[68:69], %3 offset:0\n\t" \
  "ds_read_b64_tr_b16 v[70:71], %3 offset:1024\n\t" \
  "ds_read_b64_tr_b16 v[72:73], %2 offset:4096\n\t" \
  "ds_read_b64_tr_b16 v[74:75], %2 offset:5120\n\t" \
  "ds_read_b64_tr_b16 v[76:77], %3 offset:4096\n\t" \
  "ds_read_b64_tr_b16 v[78:79], %3 offset:5120\n\t" \
  "ds_read_b64_tr_b16 v[80:81], %2 offset:8192\n\t" \
  "ds_read_b64_tr_b16 v[82:83], %2 offset:9216\n\t" \
  "ds_read_b64_tr_b16 v[84:85], %3 offset:8192\n\t" \
  "ds_read_b64_tr_b16 v[86:87], %3 offset:9216\n\t" \
  "ds_read_b64_tr_b16 v[88:89], %2 offset:12288\n\t" \
  "ds_read_b64_tr_b16 v[90:91], %2 offset:13312\n\t" \
  "ds_read_b64_tr_b16 v[92:93], %3 offset:12288\n\t" \
  "ds_read_b64_tr_b16 v[94:95], %3 offset:13312\n\t" \
  "ds_read_b64_tr_b16 v[96:97], %2 offset:16384\n\t" \
  "ds_read_b64_tr_b16 v[98:99], %2 offset:17408\n\t" \
  "ds_read_b64_tr_b16 v[100:101], %3 offset:16384\n\t" \
  "ds_read_b64_tr_b16 v[102:103], %3 offset:17408\n\t" \
  "ds_read_b64_tr_b16 v[104:105], %2 offset:20480\n\t" \
  "ds_read_b64_tr_b16 v[106:107], %2 offset:21504\n\t" \
  "ds_read_b64_tr_b16 v[108:109], %3 offset:20480\n\t" \
  "ds_read_b64_tr_b16 v[110:111], %3 offset:21504\n\t" \
  "ds_read_b64_tr_b16 v[112:113], %2 offset:24576\n\t" \
  "ds_read_b64_tr_b16 v[114:115], %2 offset:25600\n\t" \
  "ds_read_b64_tr_b16 v[116:117], %3 offset:24576\n\t" \
  "ds_read_b64_tr_b16 v[118:119], %3 offset:25600\n\t" \
  "ds_read_b64_tr_b16 v[120:121], %2 offset:28672\n\t" \
  "ds_read_b64_tr_b16 v[122:123], %2 offset:29696\n\t" \
  "ds_read_b64_tr_b16 v[124:125], %3 offset:28672\n\t" \
  "ds_read_b64_tr_b16 v[126:127], %3 offset:29696\n\t" \
  "ds_read_b64_tr_b16 v[128:129], %2 offset:32768\n\t" \
  "ds_read_b64_tr_b16 v[130:131], %2 offset:33792\n\t" \
  "ds_read_b64_tr_b16 v[132:133], %3 offset:32768\n\t" \
  "ds_read_b64_tr_b16 v[134:135], %3 offset:33792\n\t" \
  "ds_read_b64_tr_b16 v[136:137], %2 offset:36864\n\t" \
  "ds_read_b64_tr_b16 v[138:139], %2 offset:37888\n\t" \
  "ds_read_b64_tr_b16 v[140:141], %3 offset:36864\n\t" \
  "ds_read_b64_tr_b16 v[142:143], %3 offset:37888\n\t" \
  "ds_read_b64_tr_b16 v[144:145], %2 offset:40960\n\t" \
  "ds_read_b64_tr_b16 v[146:147], %2 offset:41984\n\t" \
  "ds_read_b64_tr_b16 v[148:149], %3 offset:40960\n\t" \
  "ds_read_b64_tr_b16 v[150:151], %3 offset:41984\n\t" \
  "ds_read_b64_tr_b16 v[152:153], %2 offset:45056\n\t" \
  "ds_read_b64_tr_b16 v[154:155], %2 offset:46080\n\t" \
  "ds_read_b64_tr_b16 v[156:157], %3 offset:45056\n\t" \
  "ds_read_b64_tr_b16 v[158:159], %3 offset:46080\n\t" \
  "ds_read_b64_tr_b16 v[160:161], %2 offset:49152\n\t" \
  "ds_read_b64_tr_b16 v[162:163], %2 offset:50176\n\t" \
  "ds_read_b64_tr_b16 v[164:165], %3 offset:49152\n\t" \
  "ds_read_b64_tr_b16 v[166:167], %3 offset:50176\n\t" \
  "ds_read_b64_tr_b16 v[168:169], %2 offset:53248\n\t" \
  "ds_read_b64_tr_b16 v[170:171], %2 offset:54272\n\t" \
  "ds_read_b64_tr_b16 v[172:173], %3 offset:53248\n\t" \
  "ds_read_b64_tr_b16 v[174:175], %3 offset:54272\n\t" \
  "v_mov_b32 v233, 1\n\t" \
  "s_mov_b64 exec, 1\n\t" \
  "ds_add_u32 %5, v233 offset:72\n\t" \
  "s_mov_b64 exec, -1\n\t" \
  "v_add_u32 %2, 0xe000, %2\n\t" \
  "v_add_u32 %3, 0xe000, %3\n\t" \
  "s_cmp_lg_u32 s87, 0\n\t" \
  "s_cbranch_scc1 82f\n\t" \
  "s_call_b64 s[88:89], 80f\n\t" \
  "82:\n\t" \
  "s_setprio 0\n\t" \
  "s_cmp_lg_u32 %9, 0\n\t" \
  "s_cbranch_scc1 92f\n\t" \
  "s_mov_b32 s93, 0x10000\n\t" \
  "94:\n\t" \
  "global_load_dword v232, %7, %8 sc1\n\t" \
  "s_waitcnt vmcnt(0)\n\t" \
  "v_cmp_ne_u32 vcc, %10, v232\n\t" \
  "s_cbranch_vccz 95f\n\t" \
  "s_sleep " POLLSLEEP "\n\t" \
  "s_sub_u32 s93, s93, 1\n\t" \
  "s_cbranch_scc0 94b\n\t" \
  "95:\n\t" \
  "v_mov_b32 v233, 1\n\t" \
  "s_mov_b64 exec, 1\n\t" \
  "ds_add_u32 %5, v233 offset:68\n\t" \
  "s_mov_b64 exec, -1\n\t" \
  "s_waitcnt lgkmcnt(0)\n\t" \
  "s_branch 91f\n\t" \
  "80:\n\t" \
  "v_mov_b32 v232, 1\n\t" \
  "s_waitcnt vmcnt(0)\n\t" \
  "s_mov_b64 exec, 1\n\t" \
  "ds_add_rtn_u32 v232, %5, v232 offset:32\n\t" \
  "s_waitcnt lgkmcnt(0)\n\t" \
  "v_readfirstlane_b32 s84, v232\n\t" \
  "v_mov_b32 v232, %10\n\t" \
  "s_cmp_lg_u32 s84, %13\n\t" \
  "s_cbranch_scc1 83f\n\t" \
  "global_store_dword %11, v232, %12 sc1\n\t" \
  "83:\n\t" \
  "s_mov_b64 exec, -1\n\t" \
  "s_setpc_b64 s[88:89]\n\t" \
  "92:\n\t" \
  "s_mov_b32 s93, 0x400000\n\t" \
  "102:\n\t" \
  "ds_read_b32 v232, %5 offset:72\n\t" \
  "s_waitcnt lgkmcnt(0)\n\t" \
  "v_readfirstlane_b32 s84, v232\n\t" \
  "s_sub_u32 s93, s93, 1\n\t" \
  "s_cbranch_scc1 103f\n\t" \
  "s_cmp_ge_u32 s84, 4\n\t" \
  "s_cbranch_scc1 103f\n\t" \
  "s_sleep 1\n\t" \
  "s_branch 102b\n\t" \
  "103:\n\t" \
  "s_mov_b32 s93, 0x400000\n\t" \
  "104:\n\t" \
  "ds_read_b32 v232, %5 offset:8\n\t" \
  "s_waitcnt lgkmcnt(0)\n\t" \
  "v_readfirstlane_b32 s84, v232\n\t" \
  "s_sub_u32 s93, s93, 1\n\t" \
  "s_cbranch_scc1 105f\n\t" \
  "s_cmp_ge_u32 s84, 2\n\t" \
  "s_cbranch_scc1 105f\n\t" \
  "s_sleep 1\n\t" \
  "s_branch 104b\n\t" \
  "105:\n\t" \
  "v_add_u32 v233, 0x1c00, %14\n\t" \
  "s_mov_b64 s[80:81], %15\n\t" \
  "s_add_u32 s82, s80, 0xab800\n\t" \
  "s_addc_u32 s83, s81, 0\n\t" \
  "s_mov_b32 s84, %16\n\t" \
  "s_mov_b32 m0, s84\n\t" \
  "s_add_u32 s84, s84, 0x1000\n\t" \
  "global_load_lds_dwordx4 %14, s[80:81]\n\t" \
  "global_load_lds_dwordx4 v233, s[80:81] offset:1024\n\t" \
  "global_load_lds_dwordx4 %14, s[82:83] offset:2048\n\t" \
  "global_load_lds_dwordx4 v233, s[82:83] offset:3072\n\t" \
  "s_add_u32 s80, s80, 0x4000\n\t" \
  "s_addc_u32 s81, s81, 0\n\t" \
  "s_add_u32 s82, s82, 0x4000\n\t" \
  "s_addc_u32 s83, s83, 0\n\t" \
  "s_mov_b32 m0, s84\n\t" \
  "s_add_u32 s84, s84, 0x1000\n\t" \
  "global_load_lds_dwordx4 %14, s[80:81]\n\t" \
  "global_load_lds_dwordx4 v233, s[80:81] offset:1024\n\t" \
  "global_load_lds_dwordx4 %14, s[82:83] offset:2048\n\t" \
  "global_load_lds_dwordx4 v233, s[82:83] offset:3072\n\t" \
  "s_add_u32 s80, s80, 0x4000\n\t" \
  "s_addc_u32 s81, s81, 0\n\t" \
  "s_add_u32 s82, s82, 0x4000\n\t" \
  "s_addc_u32 s83, s83, 0\n\t" \
  "s_mov_b32 m0, s84\n\t" \
  "s_add_u32 s84, s84, 0x1000\n\t" \
  "global_load_lds_dwordx4 %14, s[80:81]\n\t" \
  "global_load_lds_dwordx4 v233, s[80:81] offset:1024\n\t" \
  "global_load_lds_dwordx4 %14, s[82:83] offset:2048\n\t" \
  "global_load_lds_dwordx4 v233, s[82:83] offset:3072\n\t" \
  "s_add_u32 s80, s80, 0x4000\n\t" \
  "s_addc_u32 s81, s81, 0\n\t" \
  "s_add_u32 s82, s82, 0x4000\n\t" \
  "s_addc_u32 s83, s83, 0\n\t" \
  "s_mov_b32 m0, s84\n\t" \
  "s_add_u32 s84, s84, 0x1000\n\t" \
  "global_load_lds_dwordx4 %14, s[80:81]\n\t" \
  "global_load_lds_dwordx4 v233, s[80:81] offset:1024\n\t" \
  "global_load_lds_dwordx4 %14, s[82:83] offset:2048\n\t" \
  "global_load_lds_dwordx4 v233, s[82:83] offset:3072\n\t" \
  "s_add_u32 s80, s80, 0x4000\n\t" \
  "s_addc_u32 s81, s81, 0\n\t" \
  "s_add_u32 s82, s82, 0x4000\n\t" \
  "s_addc_u32 s83, s83, 0\n\t" \
  "s_mov_b32 m0, %17\n\t" \
  "s_add_u32 s84, s84, 0x1000\n\t" \
  "global_load_lds_dwordx4 %14, s[80:81]\n\t" \
  "global_load_lds_dwordx4 v233, s[80:81] offset:1024\n\t" \
  "global_load_lds_dwordx4 %14, s[82:83] offset:2048\n\t" \
  "global_load_lds_dwordx4 v233, s[82:83] offset:3072\n\t" \
  "s_waitcnt vmcnt(0)\n\t" \
  "v_mov_b32 v233, 1\n\t" \
  "s_mov_b64 exec, 1\n\t" \
  "ds_add_u32 %5, v233 offset:76\n\t" \
  "s_mov_b64 exec, -1\n\t" \
  "s_mov_b32 s93, 0x400000\n\t" \
  "106:\n\t" \
  "ds_read_b32 v232, %5 offset:68\n\t" \
  "s_waitcnt lgkmcnt(0)\n\t" \
  "v_readfirstlane_b32 s84, v232\n\t" \
  "s_sub_u32 s93, s93, 1\n\t" \
  "s_cbranch_scc1 107f\n\t" \
  "s_cmp_ge_u32 s84, 1\n\t" \
  "s_cbranch_scc1 107f\n\t" \
  "s_sleep 1\n\t" \
  "s_branch 106b\n\t" \
  "107:\n\t" \
  "91:\n\t" \
  "s_mov_b64 s[80:81], %6\n\t" \
  "global_load_dwordx4 v[176:179], %4, s[80:81] offset:0 sc1\n\t" \
  "global_load_dwordx4 v[180:183], %4, s[80:81] offset:1024 sc1\n\t" \
  "global_load_dwordx4 v[184:187], %4, s[80:81] offset:2048 sc1\n\t" \
  "global_load_dwordx4 v[188:191], %4, s[80:81] offset:3072 sc1\n\t" \
  "s_add_u32 s80, s80, 4096\n\t" \
  "s_addc_u32 s81, s81, 0\n\t" \
  "global_load_dwordx4 v[192:195], %4, s[80:81] offset:0 sc1\n\t" \
  "global_load_dwordx4 v[196:199], %4, s[80:81] offset:1024 sc1\n\t" \
  "global_load_dwordx4 v[200:203], %4, s[80:81] offset:2048 sc1\n\t" \
  "global_load_dwordx4 v[204:207], %4, s[80:81] offset:3072 sc1\n\t" \
  "s_add_u32 s80, s80, 4096\n\t" \
  "s_addc_u32 s81, s81, 0\n\t" \
  "global_load_dwordx4 v[208:211], %4, s[80:81] offset:0 sc1\n\t" \
  "global_load_dwordx4 v[212:215], %4, s[80:81] offset:1024 sc1\n\t" \
  "global_load_dwordx4 v[216:219], %4, s[80:81] offset:2048 sc1\n\t" \
  "global_load_dwordx4 v[220:223], %4, s[80:81] offset:3072 sc1\n\t" \
  "s_add_u32 s80, s80, 4096\n\t" \
  "s_addc_u32 s81, s81, 0\n\t" \
  "global_load_dwordx4 v[224:227], %4, s[80:81] offset:0 sc1\n\t" \
  "global_load_dwordx4 v[228:231], %4, s[80:81] offset:1024 sc1\n\t" \
  "s_add_u32 s80, s80, 2048\n\t" \
  "s_addc_u32 s81, s81, 0\n\t" \
  "s_cmp_ge_u32 s86, 52\n\t" \
  "s_cbranch_scc1 109f\n\t" \
  "s_mov_b32 s93, 0x400000\n\t" \
  "108:\n\t" \
  "ds_read_b32 v232, %5 offset:0\n\t" \
  "s_waitcnt lgkmcnt(0)\n\t" \
  "v_readfirstlane_b32 s86, v232\n\t" \
  "s_sub_u32 s93, s93, 1\n\t" \
  "s_cbranch_scc1 109f\n\t" \
  "s_cmp_ge_u32 s86, 52\n\t" \
  "s_cbranch_scc1 109f\n\t" \
  "s_sleep 1\n\t" \
  "s_branch 108b\n\t" \
  "109:\n\t" \
  "s_mov_b32 s93, 0x400000\n\t" \
  "110:\n\t" \
  "ds_read_b32 v232, %5 offset:32\n\t" \
  "s_waitcnt lgkmcnt(0)\n\t" \
  "v_readfirstlane_b32 s84, v232\n\t" \
  "s_sub_u32 s93, s93, 1\n\t" \
  "s_cbranch_scc1 111f\n\t" \
  "s_cmp_ge_u32 s84, 4\n\t" \
  "s_cbranch_scc1 111f\n\t" \
  "s_sleep 1\n\t" \
  "s_branch 110b\n\t" \
  "111:\n\t" \
  "s_setprio " G2PRIO "\n\t" \
  "50:\n\t" \
  "s_waitcnt vmcnt(13) lgkmcnt(12)\n\t" \
  "v_mfma_f32_32x32x16_bf16 %0, v[64:67], v[176:179], %0\n\t" \
  "v_mfma_f32_32x32x16_bf16 %1, v[68:71], v[176:179], %1\n\t" \
  "global_load_dwordx4 v[176:179], %4, s[80:81] offset:0 sc1\n\t" \
  "ds_read_b64_tr_b16 v[64:65], %2 offset:0\n\t" \
  "ds_read_b64_tr_b16 v[66:67], %2 offset:1024\n\t" \
  "ds_read_b64_tr_b16 v[68:69], %3 offset:0\n\t" \
  "ds_read_b64_tr_b16 v[70:71], %3 offset:1024\n\t" \
  "s_waitcnt vmcnt(13) lgkmcnt(12)\n\t" \
  "v_mfma_f32_32x32x16_bf16 %0, v[72:75], v[180:183], %0\n\t" \
  "v_mfma_f32_32x32x16_bf16 %1, v[76:79], v[180:183], %1\n\t" \
  "global_load_dwordx4 v[180:183], %4, s[80:81] offset:1024 sc1\n\t" \
  "ds_read_b64_tr_b16 v[72:73], %2 offset:4096\n\t" \
  "ds_read_b64_tr_b16 v[74:75], %2 offset:5120\n\t" \
  "ds_read_b64_tr_b16 v[76:77], %3 offset:4096\n\t" \
  "ds_read_b64_tr_b16 v[78:79], %3 offset:5120\n\t" \
  "s_waitcnt vmcnt(13) lgkmcnt(12)\n\t" \
  "v_mfma_f32_32x32x16_bf16 %0, v[80:83], v[184:187], %0\n\t" \
  "v_mfma_f32_32x32x16_bf16 %1, v[84:87], v[184:187], %1\n\t" \
  "global_load_dwordx4 v[184:187], %4, s[80:81] offset:2048 sc1\n\t" \
  "ds_read_b64_tr_b16 v[80:81], %2 offset:8192\n\t" \
  "ds_read_b64_tr_b16 v[82:83], %2 offset:9216\n\t" \
  "ds_read_b64_tr_b16 v[84:85], %3 offset:8192\n\t" \
  "ds_read_b64_tr_b16 v[86:87], %3 offset:9216\n\t" \
  "s_waitcnt vmcnt(13) lgkmcnt(12)\n\t" \
  "v_mfma_f32_32x32x16_bf16 %0, v[88:91], v[188:191], %0\n\t" \
  "v_mfma_f32_32x32x16_bf16 %1, v[92:95], v[188:191], %1\n\t" \
  "global_load_dwordx4 v[188:191], %4, s[80:81] offset:3072 sc1\n\t" \
  "s_add_u32 s80, s80, 4096\n\t" \
  "s_addc_u32 s81, s81, 0\n\t" \
  "ds_read_b64_tr_b16 v[88:89], %2 offset:12288\n\t" \
  "ds_read_b64_tr_b16 v[90:91], %2 offset:13312\n\t" \
  "ds_read_b64_tr_b16 v[92:93], %3 offset:12288\n\t" \
  "ds_read_b64_tr_b16 v[94:95], %3 offset:13312\n\t" \
  "s_waitcnt vmcnt(13) lgkmcnt(12)\n\t" \
  "v_mfma_f32_32x32x16_bf16 %0, v[96:99], v[192:195], %0\n\t" \
  "v_mfma_f32_32x32x16_bf16 %1, v[100:103], v[192:195], %1\n\t" \
  "global_load_dwordx4 v[192:195], %4, s[80:81] offset:0 sc1\n\t" \
  "ds_read_b64_tr_b16 v[96:97], %2 offset:16384\n\t" \
  "ds_read_b64_tr_b16 v[98:99], %2 offset:17408\n\t" \
  "ds_read_b64_tr_b16 v[100:101], %3 offset:16384\n\t" \
  "ds_read_b64_tr_b16 v[102:103], %3 offset:17408\n\t" \
  "s_waitcnt vmcnt(13) lgkmcnt(12)\n\t" \
  "v_mfma_f32_32x32x16_bf16 %0, v[104:107], v[196:199], %0\n\t" \
  "v_mfma_f32_32x32x16_bf16 %1, v[108:111], v[196:199], %1\n\t" \
  "global_load_dwordx4 v[196:199], %4, s[80:81] offset:1024 sc1\n\t" \
  "ds_read_b64_tr_b16 v[104:105], %2 offset:20480\n\t" \
  "ds_read_b64_tr_b16 v[106:107], %2 offset:21504\n\t" \
  "ds_read_b64_tr_b16 v[108:109], %3 offset:20480\n\t" \
  "ds_read_b64_tr_b16 v[110:111], %3 offset:21504\n\t" \
  "s_waitcnt vmcnt(13) lgkmcnt(12)\n\t" \
  "v_mfma_f32_32x32x16_bf16 %0, v[112:115], v[200:203], %0\n\t" \
  "v_mfma_f32_32x32x16_bf16 %1, v[116:119], v[200:203], %1\n\t" \
  "global_load_dwordx4 v[200:203], %4, s[80:81] offset:2048 sc1\n\t" \
  "ds_read_b64_tr_b16 v[112:113], %2 offset:24576\n\t" \
  "ds_read_b64_tr_b16 v[114:115], %2 offset:25600\n\t" \
  "ds_read_b64_tr_b16 v[116:117], %3 offset:24576\n\t" \
  "ds_read_b64_tr_b16 v[118:119], %3 offset:25600\n\t" \
  "s_waitcnt vmcnt(13) lgkmcnt(12)\n\t" \
  "v_mfma_f32_32x32x16_bf16 %0, v[120:123], v[204:207], %0\n\t" \
  "v_mfma_f32_32x32x16_bf16 %1, v[124:127], v[204:207], %1\n\t" \
  "global_load_dwordx4 v[204:207], %4, s[80:81] offset:3072 sc1\n\t" \
  "s_add_u32 s80, s80, 4096\n\t" \
  "s_addc_u32 s81, s81, 0\n\t" \
  "ds_read_b64_tr_b16 v[120:121], %2 offset:28672\n\t" \
  "ds_read_b64_tr_b16 v[122:123], %2 offset:29696\n\t" \
  "ds_read_b64_tr_b16 v[124:125], %3 offset:28672\n\t" \
  "ds_read_b64_tr_b16 v[126:127], %3 offset:29696\n\t" \
  "s_waitcnt vmcnt(13) lgkmcnt(12)\n\t" \
  "v_mfma_f32_32x32x16_bf16 %0, v[128:131], v[208:211], %0\n\t" \
  "v_mfma_f32_32x32x16_bf16 %1, v[132:135], v[208:211], %1\n\t" \
  "global_load_dwordx4 v[208:211], %4, s[80:81] offset:0 sc1\n\t" \
  "ds_read_b64_tr_b16 v[128:129], %2 offset:32768\n\t" \
  "ds_read_b64_tr_b16 v[130:131], %2 offset:33792\n\t" \
  "ds_read_b64_tr_b16 v[132:133], %3 offset:32768\n\t" \
  "ds_read_b64_tr_b16 v[134:135], %3 offset:33792\n\t" \
  "s_waitcnt vmcnt(13) lgkmcnt(12)\n\t" \
  "v_mfma_f32_32x32x16_bf16 %0, v[136:139], v[212:215], %0\n\t" \
  "v_mfma_f32_32x32x16_bf16 %1, v[140:143], v[212:215], %1\n\t" \
  "global_load_dwordx4 v[212:215], %4, s[80:81] offset:1024 sc1\n\t" \
  "ds_read_b64_tr_b16 v[136:137], %2 offset:36864\n\t" \
  "ds_read_b64_tr_b16 v[138:139], %2 offset:37888\n\t" \
  "ds_read_b64_tr_b16 v[140:141], %3 offset:36864\n\t" \
  "ds_read_b64_tr_b16 v[142:143], %3 offset:37888\n\t" \
  "s_waitcnt vmcnt(13) lgkmcnt(12)\n\t" \
  "v_mfma_f32_32x32x16_bf16 %0, v[144:147], v[216:219], %0\n\t" \
  "v_mfma_f32_32x32x16_bf16 %1, v[148:151], v[216:219], %1\n\t" \
  "global_load_dwordx4 v[216:219], %4, s[80:81] offset:2048 sc1\n\t" \
  "ds_read_b64_tr_b16 v[144:145], %2 offset:40960\n\t" \
  "ds_read_b64_tr_b16 v[146:147], %2 offset:41984\n\t" \
  "ds_read_b64_tr_b16 v[148:149], %3 offset:40960\n\t" \
  "ds_read_b64_tr_b16 v[150:151], %3 offset:41984\n\t" \
  "s_waitcnt vmcnt(13) lgkmcnt(12)\n\t" \
  "v_mfma_f32_32x32x16_bf16 %0, v[152:155], v[220:223], %0\n\t" \
  "v_mfma_f32_32x32x16_bf16 %1, v[156:159], v[220:223], %1\n\t" \
  "global_load_dwordx4 v[220:223], %4, s[80:81] offset:3072 sc1\n\t" \
  "s_add_u32 s80, s80, 4096\n\t" \
  "s_addc_u32 s81, s81, 0\n\t" \
  "ds_read_b64_tr_b16 v[152:153], %2 offset:45056\n\t" \
  "ds_read_b64_tr_b16 v[154:155], %2 offset:46080\n\t" \
  "ds_read_b64_tr_b16 v[156:157], %3 offset:45056\n\t" \
  "ds_read_b64_tr_b16 v[158:159], %3 offset:46080\n\t" \
  "s_waitcnt vmcnt(13) lgkmcnt(12)\n\t" \
  "v_mfma_f32_32x32x16_bf16 %0, v[160:163], v[224:227], %0\n\t" \
  "v_mfma_f32_32x32x16_bf16 %1, v[164:167], v[224:227], %1\n\t" \
  "global_load_dwordx4 v[224:227], %4, s[80:81] offset:0 sc1\n\t" \
  "ds_read_b64_tr_b16 v[160:161], %2 offset:49152\n\t" \
  "ds_read_b64_tr_b16 v[162:163], %2 offset:50176\n\t" \
  "ds_read_b64_tr_b16 v[164:165], %3 offset:49152\n\t" \
  "ds_read_b64_tr_b16 v[166:167], %3 offset:50176\n\t" \
  "s_waitcnt vmcnt(13) lgkmcnt(12)\n\t" \
  "v_mfma_f32_32x32x16_bf16 %0, v[168:171], v[228:231], %0\n\t" \
  "v_mfma_f32_32x32x16_bf16 %1, v[172:175], v[228:231], %1\n\t" \
  "global_load_dwordx4 v[228:231], %4, s[80:81] offset:1024 sc1\n\t" \
  "s_add_u32 s80, s80, 2048\n\t" \
  "s_addc_u32 s81, s81, 0\n\t" \
  "ds_read_b64_tr_b16 v[168:169], %2 offset:53248\n\t" \
  "ds_read_b64_tr_b16 v[170:171], %2 offset:54272\n\t" \
  "ds_read_b64_tr_b16 v[172:173], %3 offset:53248\n\t" \
  "ds_read_b64_tr_b16 v[174:175], %3 offset:54272\n\t" \
  "s_add_u32 s92, s92, 14\n\t" \
  "s_cmp_eq_u32 s92, 28\n\t" \
  "s_cbranch_scc1 51f\n\t" \
  "s_mov_b32 s93, 0x400000\n\t" \
  "112:\n\t" \
  "ds_read_b32 v232, %5 offset:76\n\t" \
  "s_waitcnt lgkmcnt(0)\n\t" \
  "v_readfirstlane_b32 s84, v232\n\t" \
  "s_sub_u32 s93, s93, 1\n\t" \
  "s_cbranch_scc1 113f\n\t" \
  "s_cmp_ge_u32 s84, 3\n\t" \
  "s_cbranch_scc1 113f\n\t" \
  "s_sleep 1\n\t" \
  "s_branch 112b\n\t" \
  "113:\n\t" \
  "v_subrev_u32 %2, 0xe000, %2\n\t" \
  "v_subrev_u32 %3, 0xe000, %3\n\t" \
  "s_branch 50b\n\t" \
  "51:\n\t" \
  "v_add_u32 %2, " W42OFF ", %2\n\t" \
  "v_add_u32 %3, " W42OFF ", %3\n\t" \
  "s_waitcnt vmcnt(13) lgkmcnt(12)\n\t" \
  "v_mfma_f32_32x32x16_bf16 %0, v[64:67], v[176:179], %0\n\t" \
  "v_mfma_f32_32x32x16_bf16 %1, v[68:71], v[176:179], %1\n\t" \
  "global_load_dwordx4 v[176:179], %4, s[80:81] sc1\n\t" \
  "ds_read_b64_tr_b16 v[64:65], %2 offset:0\n\t" \
  "ds_read_b64_tr_b16 v[66:67], %2 offset:1024\n\t" \
  "ds_read_b64_tr_b16 v[68:69], %3 offset:0\n\t" \
  "ds_read_b64_tr_b16 v[70:71], %3 offset:1024\n\t" \
  "v_mov_b32 v233, 1\n\t" \
  "s_mov_b64 exec, 1\n\t" \
  "ds_add_u32 %5, v233 offset:44\n\t" \
  "s_mov_b64 exec, -1\n\t" \
  "s_waitcnt vmcnt(13)\n\t" \
  "v_mfma_f32_32x32x16_bf16 %0, v[72:75], v[180:183], %0\n\t" \
  "v_mfma_f32_32x32x16_bf16 %1, v[76:79], v[180:183], %1\n\t" \
  "s_waitcnt vmcnt(12)\n\t" \
  "v_mfma_f32_32x32x16_bf16 %0, v[80:83], v[184:187], %0\n\t" \
  "v_mfma_f32_32x32x16_bf16 %1, v[84:87], v[184:187], %1\n\t" \
  "s_waitcnt vmcnt(11)\n\t" \
  "v_mfma_f32_32x32x16_bf16 %0, v[88:91], v[188:191], %0\n\t" \
  "v_mfma_f32_32x32x16_bf16 %1, v[92:95], v[188:191], %1\n\t" \
  "s_waitcnt vmcnt(10)\n\t" \
  "v_mfma_f32_32x32x16_bf16 %0, v[96:99], v[192:195], %0\n\t" \
  "v_mfma_f32_32x32x16_bf16 %1, v[100:103], v[192:195], %1\n\t" \
  "s_waitcnt vmcnt(9)\n\t" \
  "v_mfma_f32_32x32x16_bf16 %0, v[104:107], v[196:199], %0\n\t" \
  "v_mfma_f32_32x32x16_bf16 %1, v[108:111], v[196:199], %1\n\t" \
  "s_waitcnt vmcnt(8)\n\t" \
  "v_mfma_f32_32x32x16_bf16 %0, v[112:115], v[200:203], %0\n\t" \
  "v_mfma_f32_32x32x16_bf16 %1, v[116:119], v[200:203], %1\n\t" \
  "s_waitcnt vmcnt(7)\n\t" \
  "v_mfma_f32_32x32x16_bf16 %0, v[120:123], v[204:207], %0\n\t" \
  "v_mfma_f32_32x32x16_bf16 %1, v[124:127], v[204:207], %1\n\t" \
  "s_waitcnt vmcnt(6) lgkmcnt(0)\n\t" \
  "v_mfma_f32_32x32x16_bf16 %0, v[128:131], v[208:211], %0\n\t" \
  "v_mfma_f32_32x32x16_bf16 %1, v[132:135], v[208:211], %1\n\t" \
  "s_waitcnt vmcnt(5)\n\t" \
  "v_mfma_f32_32x32x16_bf16 %0, v[136:139], v[212:215], %0\n\t" \
  "v_mfma_f32_32x32x16_bf16 %1, v[140:143], v[212:215], %1\n\t" \
  "s_waitcnt vmcnt(4)\n\t" \
  "v_mfma_f32_32x32x16_bf16 %0, v[144:147], v[216:219], %0\n\t" \
  "v_mfma_f32_32x32x16_bf16 %1, v[148:151], v[216:219], %1\n\t" \
  "s_waitcnt vmcnt(3)\n\t" \
  "v_mfma_f32_32x32x16_bf16 %0, v[152:155], v[220:223], %0\n\t" \
  "v_mfma_f32_32x32x16_bf16 %1, v[156:159], v[220:223], %1\n\t" \
  "s_waitcnt vmcnt(2)\n\t" \
  "v_mfma_f32_32x32x16_bf16 %0, v[160:163], v[224:227], %0\n\t" \
  "v_mfma_f32_32x32x16_bf16 %1, v[164:167], v[224:227], %1\n\t" \
  "s_waitcnt vmcnt(1)\n\t" \
  "v_mfma_f32_32x32x16_bf16 %0, v[168:171], v[228:231], %0\n\t" \
  "v_mfma_f32_32x32x16_bf16 %1, v[172:175], v[228:231], %1\n\t" \
  "s_waitcnt vmcnt(0)\n\t" \
  "v_mfma_f32_32x32x16_bf16 %0, v[64:67], v[176:179], %0\n\t" \
  "v_mfma_f32_32x32x16_bf16 %1, v[68:71], v[176:179], %1\n\t" \
  "s_setprio 3\n\t" \
  "s_nop 15\n\t" \
  ""
#define G2_CLOB "v64", "v65", "v66", "v67", "v68", "v69", "v70", "v71", "v72", "v73", "v74", "v75", "v76", "v77", "v78", "v79", "v80", "v81", "v82", "v83", "v84", "v85", "v86", "v87", "v88", "v89", "v90", "v91", "v92", "v93", "v94", "v95", "v96", "v97", "v98", "v99", "v100", "v101", "v102", "v103", "v104", "v105", "v106", "v107", "v108", "v109", "v110", "v111", "v112", "v113", "v114", "v115", "v116", "v117", "v118", "v119", "v120", "v121", "v122", "v123", "v124", "v125", "v126", "v127", "v128", "v129", "v130", "v131", "v132", "v133", "v134", "v135", "v136", "v137", "v138", "v139", "v140", "v141", "v142", "v143", "v144", "v145", "v146", "v147", "v148", "v149", "v150", "v151", "v152", "v153", "v154", "v155", "v156", "v157", "v158", "v159", "v160", "v161", "v162", "v163", "v164", "v165", "v166", "v167", "v168", "v169", "v170", "v171", "v172", "v173", "v174", "v175", "v176", "v177", "v178", "v179", "v180", "v181", "v182", "v183", "v184", "v185", "v186", "v187", "v188", "v189", "v190", "v191", "v192", "v193", "v194", "v195", "v196", "v197", "v198", "v199", "v200", "v201", "v202", "v203", "v204", "v205", "v206", "v207", "v208", "v209", "v210", "v211", "v212", "v213", "v214", "v215", "v216", "v217", "v218", "v219", "v220", "v221", "v222", "v223", "v224", "v225", "v226", "v227", "v228", "v229", "v230", "v231", "v232", "v233", "s80", "s81", "s82", "s83", "s84", "s85", "s86", "s87", "s88", "s89", "s90", "s91", "s92", "s93"
#define CVA_ASM \
  "ds_read_b64_tr_b16 v[156:157], %16 offset:3872\n\t" \
  "ds_read_b64_tr_b16 v[158:159], %16 offset:3840\n\t" \
  "ds_read_b64_tr_b16 v[152:153], %16 offset:3616\n\t" \
  "ds_read_b64_tr_b16 v[154:155], %16 offset:3584\n\t" \
  "ds_read_b64_tr_b16 v[148:149], %16 offset:3360\n\t" \
  "ds_read_b64_tr_b16 v[150:151], %16 offset:3328\n\t" \
  "ds_read_b64_tr_b16 v[144:145], %16 offset:3104\n\t" \
  "ds_read_b64_tr_b16 v[146:147], %16 offset:3072\n\t" \
  "ds_read_b64_tr_b16 v[140:141], %16 offset:2848\n\t" \
  "ds_read_b64_tr_b16 v[142:143], %16 offset:2816\n\t" \
  "ds_read_b64_tr_b16 v[136:137], %16 offset:2592\n\t" \
  "ds_read_b64_tr_b16 v[138:139], %16 offset:2560\n\t" \
  "ds_read_b64_tr_b16 v[132:133], %16 offset:2336\n\t" \
  "ds_read_b64_tr_b16 v[134:135], %16 offset:2304\n\t" \
  "ds_read_b64_tr_b16 v[128:129], %16 offset:2080\n\t" \
  "ds_read_b64_tr_b16 v[130:131], %16 offset:2048\n\t" \
  "ds_read_b64_tr_b16 v[124:125], %16 offset:1824\n\t" \
  "ds_read_b64_tr_b16 v[126:127], %16 offset:1792\n\t" \
  "ds_read_b64_tr_b16 v[120:121], %16 offset:1568\n\t" \
  "ds_read_b64_tr_b16 v[122:123], %16 offset:1536\n\t" \
  "ds_read_b64_tr_b16 v[116:117], %16 offset:1312\n\t" \
  "ds_read_b64_tr_b16 v[118:119], %16 offset:1280\n\t" \
  "ds_read_b64_tr_b16 v[112:113], %16 offset:1056\n\t" \
  "ds_read_b64_tr_b16 v[114:115], %16 offset:1024\n\t" \
  "ds_read_b64_tr_b16 v[108:109], %16 offset:800\n\t" \
  "ds_read_b64_tr_b16 v[110:111], %16 offset:768\n\t" \
  "ds_read_b64_tr_b16 v[104:105], %16 offset:544\n\t" \
  "ds_read_b64_tr_b16 v[106:107], %16 offset:512\n\t" \
  "ds_read_b64_tr_b16 v[100:101], %16 offset:288\n\t" \
  "ds_read_b64_tr_b16 v[102:103], %16 offset:256\n\t" \
  "ds_read_b64_tr_b16 v[96:97], %16 offset:32\n\t" \
  "ds_read_b64_tr_b16 v[98:99], %16 offset:0\n\t" \
  "ds_read_b64 v[160:161], %17 offset:0\n\t" \
  "ds_read_b64 v[162:163], %17 offset:8\n\t" \
  "ds_read_b64 v[164:165], %17 offset:64\n\t" \
  "ds_read_b64 v[166:167], %17 offset:72\n\t" \
  "ds_read_b64 v[168:169], %17 offset:128\n\t" \
  "ds_read_b64 v[170:171], %17 offset:136\n\t" \
  "ds_read_b64 v[172:173], %17 offset:192\n\t" \
  "ds_read_b64 v[174:175], %17 offset:200\n\t" \
  "ds_read_b64 v[176:177], %17 offset:256\n\t" \
  "ds_read_b64 v[178:179], %17 offset:264\n\t" \
  "ds_read_b64 v[180:181], %17 offset:320\n\t" \
  "ds_read_b64 v[182:183], %17 offset:328\n\t" \
  "s_waitcnt lgkmcnt(10)\n\t" \
  "v_mfma_f32_16x16x32_bf16 %3, v[156:159], v[160:163], 0\n\t" \
  "ds_read_b64 v[184:185], %17 offset:384\n\t" \
  "v_mfma_f32_16x16x32_bf16 %2, v[148:151], v[160:163], 0\n\t" \
  "ds_read_b64 v[186:187], %17 offset:392\n\t" \
  "v_mfma_f32_16x16x32_bf16 %1, v[108:111], v[160:163], 0\n\t" \
  "v_mfma_f32_16x16x32_bf16 %0, v[100:103], v[160:163], 0\n\t" \
  "s_waitcnt lgkmcnt(10)\n\t" \
  "v_mfma_f32_16x16x32_bf16 %3, v[152:155], v[164:167], %3\n\t" \
  "ds_read_b64 v[188:189], %17 offset:448\n\t" \
  "v_mfma_f32_16x16x32_bf16 %2, v[144:147], v[164:167], %2\n\t" \
  "ds_read_b64 v[190:191], %17 offset:456\n\t" \
  "v_mfma_f32_16x16x32_bf16 %1, v[104:107], v[164:167], %1\n\t" \
  "v_mfma_f32_16x16x32_bf16 %0, v[96:99], v[164:167], %0\n\t" \
  "s_waitcnt lgkmcnt(10)\n\t" \
  "v_mfma_f32_16x16x32_bf16 %3, v[148:151], v[168:171], %3\n\t" \
  "ds_read_b64 v[160:161], %17 offset:512\n\t" \
  "v_mfma_f32_16x16x32_bf16 %2, v[140:143], v[168:171], %2\n\t" \
  "ds_read_b64 v[162:163], %17 offset:520\n\t" \
  "v_mfma_f32_16x16x32_bf16 %1, v[100:103], v[168:171], %1\n\t" \
  "s_waitcnt lgkmcnt(10)\n\t" \
  "v_mfma_f32_16x16x32_bf16 %3, v[144:147], v[172:175], %3\n\t" \
  "ds_read_b64 v[164:165], %17 offset:576\n\t" \
  "v_mfma_f32_16x16x32_bf16 %2, v[136:139], v[172:175], %2\n\t" \
  "ds_read_b64 v[166:167], %17 offset:584\n\t" \
  "v_mfma_f32_16x16x32_bf16 %1, v[96:99], v[172:175], %1\n\t" \
  "s_waitcnt lgkmcnt(10)\n\t" \
  "v_mfma_f32_16x16x32_bf16 %3, v[140:143], v[176:179], %3\n\t" \
  "ds_read_b64 v[168:169], %17 offset:640\n\t" \
  "v_mfma_f32_16x16x32_bf16 %2, v[132:135], v[176:179], %2\n\t" \
  "ds_read_b64 v[170:171], %17 offset:648\n\t" \
  "s_waitcnt lgkmcnt(10)\n\t" \
  "v_mfma_f32_16x16x32_bf16 %3, v[136:139], v[180:183], %3\n\t" \
  "ds_read_b64 v[172:173], %17 offset:704\n\t" \
  "v_mfma_f32_16x16x32_bf16 %2, v[128:131], v[180:183], %2\n\t" \
  "ds_read_b64 v[174:175], %17 offset:712\n\t" \
  "s_waitcnt lgkmcnt(10)\n\t" \
  "v_mfma_f32_16x16x32_bf16 %3, v[132:135], v[184:187], %3\n\t" \
  "ds_read_b64 v[176:177], %17 offset:768\n\t" \
  "v_mfma_f32_16x16x32_bf16 %2, v[124:127], v[184:187], %2\n\t" \
  "ds_read_b64 v[178:179], %17 offset:776\n\t" \
  "s_waitcnt lgkmcnt(10)\n\t" \
  "v_mfma_f32_16x16x32_bf16 %3, v[128:131], v[188:191], %3\n\t" \
  "ds_read_b64 v[180:181], %17 offset:832\n\t" \
  "v_mfma_f32_16x16x32_bf16 %2, v[120:123], v[188:191], %2\n\t" \
  "ds_read_b64 v[182:183], %17 offset:840\n\t" \
  "s_waitcnt lgkmcnt(10)\n\t" \
  "v_mfma_f32_16x16x32_bf16 %3, v[124:127], v[160:163], %3\n\t" \
  "ds_read_b64 v[184:185], %17 offset:896\n\t" \
  "v_mfma_f32_16x16x32_bf16 %2, v[116:119], v[160:163], %2\n\t" \
  "ds_read_b64 v[186:187], %17 offset:904\n\t" \
  "s_waitcnt lgkmcnt(10)\n\t" \
  "v_mfma_f32_16x16x32_bf16 %3, v[120:123], v[164:167], %3\n\t" \
  "ds_read_b64 v[188:189], %17 offset:960\n\t" \
  "v_mfma_f32_16x16x32_bf16 %2, v[112:115], v[164:167], %2\n\t" \
  "ds_read_b64 v[190:191], %17 offset:968\n\t" \
  "s_waitcnt lgkmcnt(10)\n\t" \
  "v_mfma_f32_16x16x32_bf16 %3, v[116:119], v[168:171], %3\n\t" \
  "ds_read_b64 v[160:161], %17 offset:1168\n\t" \
  "v_mfma_f32_16x16x32_bf16 %2, v[108:111], v[168:171], %2\n\t" \
  "ds_read_b64 v[162:163], %17 offset:1176\n\t" \
  "s_waitcnt lgkmcnt(10)\n\t" \
  "v_mfma_f32_16x16x32_bf16 %3, v[112:115], v[172:175], %3\n\t" \
  "ds_read_b64 v[164:165], %17 offset:1232\n\t" \
  "v_mfma_f32_16x16x32_bf16 %2, v[104:107], v[172:175], %2\n\t" \
  "ds_read_b64 v[166:167], %17 offset:1240\n\t" \
  "s_waitcnt lgkmcnt(10)\n\t" \
  "v_mfma_f32_16x16x32_bf16 %3, v[108:111], v[176:179], %3\n\t" \
  "ds_read_b64 v[168:169], %17 offset:1296\n\t" \
  "v_mfma_f32_16x16x32_bf16 %2, v[100:103], v[176:179], %2\n\t" \
  "ds_read_b64 v[170:171], %17 offset:1304\n\t" \
  "s_waitcnt lgkmcnt(10)\n\t" \
  "v_mfma_f32_16x16x32_bf16 %3, v[104:107], v[180:183], %3\n\t" \
  "ds_read_b64 v[172:173], %17 offset:1360\n\t" \
  "v_mfma_f32_16x16x32_bf16 %2, v[96:99], v[180:183], %2\n\t" \
  "ds_read_b64 v[174:175], %17 offset:1368\n\t" \
  "s_waitcnt lgkmcnt(10)\n\t" \
  "v_mfma_f32_16x16x32_bf16 %3, v[100:103], v[184:187], %3\n\t" \
  "ds_read_b64 v[176:177], %17 offset:1424\n\t" \
  "ds_read_b64 v[178:179], %17 offset:1432\n\t" \
  "s_waitcnt lgkmcnt(10)\n\t" \
  "v_mfma_f32_16x16x32_bf16 %3, v[96:99], v[188:191], %3\n\t" \
  "ds_read_b64 v[180:181], %17 offset:1488\n\t" \
  "ds_read_b64 v[182:183], %17 offset:1496\n\t" \
  "s_waitcnt lgkmcnt(10)\n\t" \
  "v_mfma_f32_16x16x32_bf16 %7, v[156:159], v[160:163], 0\n\t" \
  "ds_read_b64 v[184:185], %17 offset:1552\n\t" \
  "v_mfma_f32_16x16x32_bf16 %6, v[148:151], v[160:163], 0\n\t" \
  "ds_read_b64 v[186:187], %17 offset:1560\n\t" \
  "v_mfma_f32_16x16x32_bf16 %5, v[108:111], v[160:163], 0\n\t" \
  "v_mfma_f32_16x16x32_bf16 %4, v[100:103], v[160:163], 0\n\t" \
  "s_waitcnt lgkmcnt(10)\n\t" \
  "v_mfma_f32_16x16x32_bf16 %7, v[152:155], v[164:167], %7\n\t" \
  "ds_read_b64 v[188:189], %17 offset:1616\n\t" \
  "v_mfma_f32_16x16x32_bf16 %6, v[144:147], v[164:167], %6\n\t" \
  "ds_read_b64 v[190:191], %17 offset:1624\n\t" \
  "v_mfma_f32_16x16x32_bf16 %5, v[104:107], v[164:167], %5\n\t" \
  "v_mfma_f32_16x16x32_bf16 %4, v[96:99], v[164:167], %4\n\t" \
  "s_waitcnt lgkmcnt(10)\n\t" \
  "v_mfma_f32_16x16x32_bf16 %7, v[148:151], v[168:171], %7\n\t" \
  "ds_read_b64 v[160:161], %17 offset:1680\n\t" \
  "v_mfma_f32_16x16x32_bf16 %6, v[140:143], v[168:171], %6\n\t" \
  "ds_read_b64 v[162:163], %17 offset:1688\n\t" \
  "v_mfma_f32_16x16x32_bf16 %5, v[100:103], v[168:171], %5\n\t" \
  "s_waitcnt lgkmcnt(10)\n\t" \
  "v_mfma_f32_16x16x32_bf16 %7, v[144:147], v[172:175], %7\n\t" \
  "ds_read_b64 v[164:165], %17 offset:1744\n\t" \
  "v_mfma_f32_16x16x32_bf16 %6, v[136:139], v[172:175], %6\n\t" \
  "ds_read_b64 v[166:167], %17 offset:1752\n\t" \
  "v_mfma_f32_16x16x32_bf16 %5, v[96:99], v[172:175], %5\n\t" \
  "s_waitcnt lgkmcnt(10)\n\t" \
  "v_mfma_f32_16x16x32_bf16 %7, v[140:143], v[176:179], %7\n\t" \
  "ds_read_b64 v[168:169], %17 offset:1808\n\t" \
  "v_mfma_f32_16x16x32_bf16 %6, v[132:135], v[176:179], %6\n\t" \
  "ds_read_b64 v[170:171], %17 offset:1816\n\t" \
  "s_waitcnt lgkmcnt(10)\n\t" \
  "v_mfma_f32_16x16x32_bf16 %7, v[136:139], v[180:183], %7\n\t" \
  "ds_read_b64 v[172:173], %17 offset:1872\n\t" \
  "v_mfma_f32_16x16x32_bf16 %6, v[128:131], v[180:183], %6\n\t" \
  "ds_read_b64 v[174:175], %17 offset:1880\n\t" \
  "s_waitcnt lgkmcnt(10)\n\t" \
  "v_mfma_f32_16x16x32_bf16 %7, v[132:135], v[184:187], %7\n\t" \
  "ds_read_b64 v[176:177], %17 offset:1936\n\t" \
  "v_mfma_f32_16x16x32_bf16 %6, v[124:127], v[184:187], %6\n\t" \
  "ds_read_b64 v[178:179], %17 offset:1944\n\t" \
  "s_waitcnt lgkmcnt(10)\n\t" \
  "v_mfma_f32_16x16x32_bf16 %7, v[128:131], v[188:191], %7\n\t" \
  "ds_read_b64 v[180:181], %17 offset:2000\n\t" \
  "v_mfma_f32_16x16x32_bf16 %6, v[120:123], v[188:191], %6\n\t" \
  "ds_read_b64 v[182:183], %17 offset:2008\n\t" \
  "s_waitcnt lgkmcnt(10)\n\t" \
  "v_mfma_f32_16x16x32_bf16 %7, v[124:127], v[160:163], %7\n\t" \
  "ds_read_b64 v[184:185], %17 offset:2064\n\t" \
  "v_mfma_f32_16x16x32_bf16 %6, v[116:119], v[160:163], %6\n\t" \
  "ds_read_b64 v[186:187], %17 offset:2072\n\t" \
  "s_waitcnt lgkmcnt(10)\n\t" \
  "v_mfma_f32_16x16x32_bf16 %7, v[120:123], v[164:167], %7\n\t" \
  "ds_read_b64 v[188:189], %17 offset:2128\n\t" \
  "v_mfma_f32_16x16x32_bf16 %6, v[112:115], v[164:167], %6\n\t" \
  "ds_read_b64 v[190:191], %17 offset:2136\n\t" \
  "s_waitcnt lgkmcnt(10)\n\t" \
  "v_mfma_f32_16x16x32_bf16 %7, v[116:119], v[168:171], %7\n\t" \
  "ds_read_b64 v[160:161], %17 offset:2336\n\t" \
  "v_mfma_f32_16x16x32_bf16 %6, v[108:111], v[168:171], %6\n\t" \
  "ds_read_b64 v[162:163], %17 offset:2344\n\t" \
  "s_waitcnt lgkmcnt(10)\n\t" \
  "v_mfma_f32_16x16x32_bf16 %7, v[112:115], v[172:175], %7\n\t" \
  "ds_read_b64 v[164:165], %17 offset:2400\n\t" \
  "v_mfma_f32_16x16x32_bf16 %6, v[104:107], v[172:175], %6\n\t" \
  "ds_read_b64 v[166:167], %17 offset:2408\n\t" \
  "s_waitcnt lgkmcnt(10)\n\t" \
  "v_mfma_f32_16x16x32_bf16 %7, v[108:111], v[176:179], %7\n\t" \
  "ds_read_b64 v[168:169], %17 offset:2464\n\t" \
  "v_mfma_f32_16x16x32_bf16 %6, v[100:103], v[176:179], %6\n\t" \
  "ds_read_b64 v[170:171], %17 offset:2472\n\t" \
  "s_waitcnt lgkmcnt(10)\n\t" \
  "v_mfma_f32_16x16x32_bf16 %7, v[104:107], v[180:183], %7\n\t" \
  "ds_read_b64 v[172:173], %17 offset:2528\n\t" \
  "v_mfma_f32_16x16x32_bf16 %6, v[96:99], v[180:183], %6\n\t" \
  "ds_read_b64 v[174:175], %17 offset:2536\n\t" \
  "s_waitcnt lgkmcnt(10)\n\t" \
  "v_mfma_f32_16x16x32_bf16 %7, v[100:103], v[184:187], %7\n\t" \
  "ds_read_b64 v[176:177], %17 offset:2592\n\t" \
  "ds_read_b64 v[178:179], %17 offset:2600\n\t" \
  "s_waitcnt lgkmcnt(10)\n\t" \
  "v_mfma_f32_16x16x32_bf16 %7, v[96:99], v[188:191], %7\n\t" \
  "ds_read_b64 v[180:181], %17 offset:2656\n\t" \
  "ds_read_b64 v[182:183], %17 offset:2664\n\t" \
  "s_waitcnt lgkmcnt(10)\n\t" \
  "v_mfma_f32_16x16x32_bf16 %11, v[156:159], v[160:163], 0\n\t" \
  "ds_read_b64 v[184:185], %17 offset:2720\n\t" \
  "v_mfma_f32_16x16x32_bf16 %10, v[148:151], v[160:163], 0\n\t" \
  "ds_read_b64 v[186:187], %17 offset:2728\n\t" \
  "v_mfma_f32_16x16x32_bf16 %9, v[108:111], v[160:163], 0\n\t" \
  "v_mfma_f32_16x16x32_bf16 %8, v[100:103], v[160:163], 0\n\t" \
  "s_waitcnt lgkmcnt(10)\n\t" \
  "v_mfma_f32_16x16x32_bf16 %11, v[152:155], v[164:167], %11\n\t" \
  "ds_read_b64 v[188:189], %17 offset:2784\n\t" \
  "v_mfma_f32_16x16x32_bf16 %10, v[144:147], v[164:167], %10\n\t" \
  "ds_read_b64 v[190:191], %17 offset:2792\n\t" \
  "v_mfma_f32_16x16x32_bf16 %9, v[104:107], v[164:167], %9\n\t" \
  "v_mfma_f32_16x16x32_bf16 %8, v[96:99], v[164:167], %8\n\t" \
  "s_waitcnt lgkmcnt(10)\n\t" \
  "v_mfma_f32_16x16x32_bf16 %11, v[148:151], v[168:171], %11\n\t" \
  "ds_read_b64 v[160:161], %17 offset:2848\n\t" \
  "v_mfma_f32_16x16x32_bf16 %10, v[140:143], v[168:171], %10\n\t" \
  "ds_read_b64 v[162:163], %17 offset:2856\n\t" \
  "v_mfma_f32_16x16x32_bf16 %9, v[100:103], v[168:171], %9\n\t" \
  "s_waitcnt lgkmcnt(10)\n\t" \
  "v_mfma_f32_16x16x32_bf16 %11, v[144:147], v[172:175], %11\n\t" \
  "ds_read_b64 v[164:165], %17 offset:2912\n\t" \
  "v_mfma_f32_16x16x32_bf16 %10, v[136:139], v[172:175], %10\n\t" \
  "ds_read_b64 v[166:167], %17 offset:2920\n\t" \
  "v_mfma_f32_16x16x32_bf16 %9, v[96:99], v[172:175], %9\n\t" \
  "s_waitcnt lgkmcnt(10)\n\t" \
  "v_mfma_f32_16x16x32_bf16 %11, v[140:143], v[176:179], %11\n\t" \
  "ds_read_b64 v[168:169], %17 offset:2976\n\t" \
  "v_mfma_f32_16x16x32_bf16 %10, v[132:135], v[176:179], %10\n\t" \
  "ds_read_b64 v[170:171], %17 offset:2984\n\t" \
  "s_waitcnt lgkmcnt(10)\n\t" \
  "v_mfma_f32_16x16x32_bf16 %11, v[136:139], v[180:183], %11\n\t" \
  "ds_read_b64 v[172:173], %17 offset:3040\n\t" \
  "v_mfma_f32_16x16x32_bf16 %10, v[128:131], v[180:183], %10\n\t" \
  "ds_read_b64 v[174:175], %17 offset:3048\n\t" \
  "s_waitcnt lgkmcnt(10)\n\t" \
  "v_mfma_f32_16x16x32_bf16 %11, v[132:135], v[184:187], %11\n\t" \
  "ds_read_b64 v[176:177], %17 offset:3104\n\t" \
  "v_mfma_f32_16x16x32_bf16 %10, v[124:127], v[184:187], %10\n\t" \
  "ds_read_b64 v[178:179], %17 offset:3112\n\t" \
  "s_waitcnt lgkmcnt(10)\n\t" \
  "v_mfma_f32_16x16x32_bf16 %11, v[128:131], v[188:191], %11\n\t" \
  "ds_read_b64 v[180:181], %17 offset:3168\n\t" \
  "v_mfma_f32_16x16x32_bf16 %10, v[120:123], v[188:191], %10\n\t" \
  "ds_read_b64 v[182:183], %17 offset:3176\n\t" \
  "s_waitcnt lgkmcnt(10)\n\t" \
  "v_mfma_f32_16x16x32_bf16 %11, v[124:127], v[160:163], %11\n\t" \
  "ds_read_b64 v[184:185], %17 offset:3232\n\t" \
  "v_mfma_f32_16x16x32_bf16 %10, v[116:119], v[160:163], %10\n\t" \
  "ds_read_b64 v[186:187], %17 offset:3240\n\t" \
  "s_waitcnt lgkmcnt(10)\n\t" \
  "v_mfma_f32_16x16x32_bf16 %11, v[120:123], v[164:167], %11\n\t" \
  "ds_read_b64 v[188:189], %17 offset:3296\n\t" \
  "v_mfma_f32_16x16x32_bf16 %10, v[112:115], v[164:167], %10\n\t" \
  "ds_read_b64 v[190:191], %17 offset:3304\n\t" \
  "s_waitcnt lgkmcnt(10)\n\t" \
  "v_mfma_f32_16x16x32_bf16 %11, v[116:119], v[168:171], %11\n\t" \
  "ds_read_b64 v[160:161], %17 offset:3504\n\t" \
  "v_mfma_f32_16x16x32_bf16 %10, v[108:111], v[168:171], %10\n\t" \
  "ds_read_b64 v[162:163], %17 offset:3512\n\t" \
  "s_waitcnt lgkmcnt(10)\n\t" \
  "v_mfma_f32_16x16x32_bf16 %11, v[112:115], v[172:175], %11\n\t" \
  "ds_read_b64 v[164:165], %17 offset:3568\n\t" \
  "v_mfma_f32_16x16x32_bf16 %10, v[104:107], v[172:175], %10\n\t" \
  "ds_read_b64 v[166:167], %17 offset:3576\n\t" \
  "s_waitcnt lgkmcnt(10)\n\t" \
  "v_mfma_f32_16x16x32_bf16 %11, v[108:111], v[176:179], %11\n\t" \
  "ds_read_b64 v[168:169], %17 offset:3632\n\t" \
  "v_mfma_f32_16x16x32_bf16 %10, v[100:103], v[176:179], %10\n\t" \
  "ds_read_b64 v[170:171], %17 offset:3640\n\t" \
  "s_waitcnt lgkmcnt(10)\n\t" \
  "v_mfma_f32_16x16x32_bf16 %11, v[104:107], v[180:183], %11\n\t" \
  "ds_read_b64 v[172:173], %17 offset:3696\n\t" \
  "v_mfma_f32_16x16x32_bf16 %10, v[96:99], v[180:183], %10\n\t" \
  "ds_read_b64 v[174:175], %17 offset:3704\n\t" \
  "s_waitcnt lgkmcnt(10)\n\t" \
  "v_mfma_f32_16x16x32_bf16 %11, v[100:103], v[184:187], %11\n\t" \
  "ds_read_b64 v[176:177], %17 offset:3760\n\t" \
  "ds_read_b64 v[178:179], %17 offset:3768\n\t" \
  "s_waitcnt lgkmcnt(10)\n\t" \
  "v_mfma_f32_16x16x32_bf16 %11, v[96:99], v[188:191], %11\n\t" \
  "ds_read_b64 v[180:181], %17 offset:3824\n\t" \
  "ds_read_b64 v[182:183], %17 offset:3832\n\t" \
  "s_waitcnt lgkmcnt(10)\n\t" \
  "v_mfma_f32_16x16x32_bf16 %15, v[156:159], v[160:163], 0\n\t" \
  "ds_read_b64 v[184:185], %17 offset:3888\n\t" \
  "v_mfma_f32_16x16x32_bf16 %14, v[148:151], v[160:163], 0\n\t" \
  "ds_read_b64 v[186:187], %17 offset:3896\n\t" \
  "v_mfma_f32_16x16x32_bf16 %13, v[108:111], v[160:163], 0\n\t" \
  "v_mfma_f32_16x16x32_bf16 %12, v[100:103], v[160:163], 0\n\t" \
  "s_waitcnt lgkmcnt(10)\n\t" \
  "v_mfma_f32_16x16x32_bf16 %15, v[152:155], v[164:167], %15\n\t" \
  "ds_read_b64 v[188:189], %17 offset:3952\n\t" \
  "v_mfma_f32_16x16x32_bf16 %14, v[144:147], v[164:167], %14\n\t" \
  "ds_read_b64 v[190:191], %17 offset:3960\n\t" \
  "v_mfma_f32_16x16x32_bf16 %13, v[104:107], v[164:167], %13\n\t" \
  "v_mfma_f32_16x16x32_bf16 %12, v[96:99], v[164:167], %12\n\t" \
  "s_waitcnt lgkmcnt(10)\n\t" \
  "v_mfma_f32_16x16x32_bf16 %15, v[148:151], v[168:171], %15\n\t" \
  "ds_read_b64 v[160:161], %17 offset:4016\n\t" \
  "v_mfma_f32_16x16x32_bf16 %14, v[140:143], v[168:171], %14\n\t" \
  "ds_read_b64 v[162:163], %17 offset:4024\n\t" \
  "v_mfma_f32_16x16x32_bf16 %13, v[100:103], v[168:171], %13\n\t" \
  "s_waitcnt lgkmcnt(10)\n\t" \
  "v_mfma_f32_16x16x32_bf16 %15, v[144:147], v[172:175], %15\n\t" \
  "ds_read_b64 v[164:165], %17 offset:4080\n\t" \
  "v_mfma_f32_16x16x32_bf16 %14, v[136:139], v[172:175], %14\n\t" \
  "ds_read_b64 v[166:167], %17 offset:4088\n\t" \
  "v_mfma_f32_16x16x32_bf16 %13, v[96:99], v[172:175], %13\n\t" \
  "s_waitcnt lgkmcnt(10)\n\t" \
  "v_mfma_f32_16x16x32_bf16 %15, v[140:143], v[176:179], %15\n\t" \
  "ds_read_b64 v[168:169], %17 offset:4144\n\t" \
  "v_mfma_f32_16x16x32_bf16 %14, v[132:135], v[176:179], %14\n\t" \
  "ds_read_b64 v[170:171], %17 offset:4152\n\t" \
  "s_waitcnt lgkmcnt(10)\n\t" \
  "v_mfma_f32_16x16x32_bf16 %15, v[136:139], v[180:183], %15\n\t" \
  "ds_read_b64 v[172:173], %17 offset:4208\n\t" \
  "v_mfma_f32_16x16x32_bf16 %14, v[128:131], v[180:183], %14\n\t" \
  "ds_read_b64 v[174:175], %17 offset:4216\n\t" \
  "s_waitcnt lgkmcnt(10)\n\t" \
  "v_mfma_f32_16x16x32_bf16 %15, v[132:135], v[184:187], %15\n\t" \
  "ds_read_b64 v[176:177], %17 offset:4272\n\t" \
  "v_mfma_f32_16x16x32_bf16 %14, v[124:127], v[184:187], %14\n\t" \
  "ds_read_b64 v[178:179], %17 offset:4280\n\t" \
  "s_waitcnt lgkmcnt(10)\n\t" \
  "v_mfma_f32_16x16x32_bf16 %15, v[128:131], v[188:191], %15\n\t" \
  "ds_read_b64 v[180:181], %17 offset:4336\n\t" \
  "v_mfma_f32_16x16x32_bf16 %14, v[120:123], v[188:191], %14\n\t" \
  "ds_read_b64 v[182:183], %17 offset:4344\n\t" \
  "s_waitcnt lgkmcnt(10)\n\t" \
  "v_mfma_f32_16x16x32_bf16 %15, v[124:127], v[160:163], %15\n\t" \
  "ds_read_b64 v[184:185], %17 offset:4400\n\t" \
  "v_mfma_f32_16x16x32_bf16 %14, v[116:119], v[160:163], %14\n\t" \
  "ds_read_b64 v[186:187], %17 offset:4408\n\t" \
  "s_waitcnt lgkmcnt(10)\n\t" \
  "v_mfma_f32_16x16x32_bf16 %15, v[120:123], v[164:167], %15\n\t" \
  "ds_read_b64 v[188:189], %17 offset:4464\n\t" \
  "v_mfma_f32_16x16x32_bf16 %14, v[112:115], v[164:167], %14\n\t" \
  "ds_read_b64 v[190:191], %17 offset:4472\n\t" \
  "s_waitcnt lgkmcnt(10)\n\t" \
  "v_mfma_f32_16x16x32_bf16 %15, v[116:119], v[168:171], %15\n\t" \
  "v_mfma_f32_16x16x32_bf16 %14, v[108:111], v[168:171], %14\n\t" \
  "s_waitcnt lgkmcnt(8)\n\t" \
  "v_mfma_f32_16x16x32_bf16 %15, v[112:115], v[172:175], %15\n\t" \
  "v_mfma_f32_16x16x32_bf16 %14, v[104:107], v[172:175], %14\n\t" \
  "s_waitcnt lgkmcnt(6)\n\t" \
  "v_mfma_f32_16x16x32_bf16 %15, v[108:111], v[176:179], %15\n\t" \
  "v_mfma_f32_16x16x32_bf16 %14, v[100:103], v[176:179], %14\n\t" \
  "s_waitcnt lgkmcnt(4)\n\t" \
  "v_mfma_f32_16x16x32_bf16 %15, v[104:107], v[180:183], %15\n\t" \
  "v_mfma_f32_16x16x32_bf16 %14, v[96:99], v[180:183], %14\n\t" \
  "s_waitcnt lgkmcnt(2)\n\t" \
  "v_mfma_f32_16x16x32_bf16 %15, v[100:103], v[184:187], %15\n\t" \
  "s_waitcnt lgkmcnt(0)\n\t" \
  "v_mfma_f32_16x16x32_bf16 %15, v[96:99], v[188:191], %15\n\t" \
  "s_nop 7\n\t"
#define CVA_CLOB "v96", "v97", "v98", "v99", "v100", "v101", "v102", "v103", "v104", "v105", "v106", "v107", "v108", "v109", "v110", "v111", "v112", "v113", "v114", "v115", "v116", "v117", "v118", "v119", "v120", "v121", "v122", "v123", "v124", "v125", "v126", "v127", "v128", "v129", "v130", "v131", "v132", "v133", "v134", "v135", "v136", "v137", "v138", "v139", "v140", "v141", "v142", "v143", "v144", "v145", "v146", "v147", "v148", "v149", "v150", "v151", "v152", "v153", "v154", "v155", "v156", "v157", "v158", "v159", "v160", "v161", "v162", "v163", "v164", "v165", "v166", "v167", "v168", "v169", "v170", "v171", "v172", "v173", "v174", "v175", "v176", "v177", "v178", "v179", "v180", "v181", "v182", "v183", "v184", "v185", "v186", "v187", "v188", "v189", "v190", "v191"
#define CVB_ASM \
  "ds_read_b64_tr_b16 v[140:141], %16 offset:2848\n\t" \
  "ds_read_b64_tr_b16 v[142:143], %16 offset:2816\n\t" \
  "ds_read_b64_tr_b16 v[136:137], %16 offset:2592\n\t" \
  "ds_read_b64_tr_b16 v[138:139], %16 offset:2560\n\t" \
  "ds_read_b64_tr_b16 v[132:133], %16 offset:2336\n\t" \
  "ds_read_b64_tr_b16 v[134:135], %16 offset:2304\n\t" \
  "ds_read_b64_tr_b16 v[128:129], %16 offset:2080\n\t" \
  "ds_read_b64_tr_b16 v[130:131], %16 offset:2048\n\t" \
  "ds_read_b64_tr_b16 v[124:125], %16 offset:1824\n\t" \
  "ds_read_b64_tr_b16 v[126:127], %16 offset:1792\n\t" \
  "ds_read_b64_tr_b16 v[120:121], %16 offset:1568\n\t" \
  "ds_read_b64_tr_b16 v[122:123], %16 offset:1536\n\t" \
  "ds_read_b64_tr_b16 v[116:117], %16 offset:1312\n\t" \
  "ds_read_b64_tr_b16 v[118:119], %16 offset:1280\n\t" \
  "ds_read_b64_tr_b16 v[112:113], %16 offset:1056\n\t" \
  "ds_read_b64_tr_b16 v[114:115], %16 offset:1024\n\t" \
  "ds_read_b64_tr_b16 v[108:109], %16 offset:800\n\t" \
  "ds_read_b64_tr_b16 v[110:111], %16 offset:768\n\t" \
  "ds_read_b64_tr_b16 v[104:105], %16 offset:544\n\t" \
  "ds_read_b64_tr_b16 v[106:107], %16 offset:512\n\t" \
  "ds_read_b64_tr_b16 v[100:101], %16 offset:288\n\t" \
  "ds_read_b64_tr_b16 v[102:103], %16 offset:256\n\t" \
  "ds_read_b64_tr_b16 v[96:97], %16 offset:32\n\t" \
  "ds_read_b64_tr_b16 v[98:99], %16 offset:0\n\t" \
  "ds_read_b64 v[144:145], %17 offset:0\n\t" \
  "ds_read_b64 v[146:147], %17 offset:8\n\t" \
  "ds_read_b64 v[148:149], %17 offset:64\n\t" \
  "ds_read_b64 v[150:151], %17 offset:72\n\t" \
  "ds_read_b64 v[152:153], %17 offset:128\n\t" \
  "ds_read_b64 v[154:155], %17 offset:136\n\t" \
  "ds_read_b64 v[156:157], %17 offset:192\n\t" \
  "ds_read_b64 v[158:159], %17 offset:200\n\t" \
  "ds_read_b64 v[160:161], %17 offset:256\n\t" \
  "ds_read_b64 v[162:163], %17 offset:264\n\t" \
  "ds_read_b64 v[164:165], %17 offset:320\n\t" \
  "ds_read_b64 v[166:167], %17 offset:328\n\t" \
  "s_waitcnt lgkmcnt(10)\n\t" \
  "v_mfma_f32_16x16x32_bf16 %3, v[140:143], v[144:147], 0\n\t" \
  "ds_read_b64 v[168:169], %17 offset:384\n\t" \
  "v_mfma_f32_16x16x32_bf16 %2, v[132:135], v[144:147], 0\n\t" \
  "ds_read_b64 v[170:171], %17 offset:392\n\t" \
  "v_mfma_f32_16x16x32_bf16 %1, v[124:127], v[144:147], 0\n\t" \
  "v_mfma_f32_16x16x32_bf16 %0, v[116:119], v[144:147], 0\n\t" \
  "s_waitcnt lgkmcnt(10)\n\t" \
  "v_mfma_f32_16x16x32_bf16 %3, v[136:139], v[148:151], %3\n\t" \
  "ds_read_b64 v[172:173], %17 offset:448\n\t" \
  "v_mfma_f32_16x16x32_bf16 %2, v[128:131], v[148:151], %2\n\t" \
  "ds_read_b64 v[174:175], %17 offset:456\n\t" \
  "v_mfma_f32_16x16x32_bf16 %1, v[120:123], v[148:151], %1\n\t" \
  "v_mfma_f32_16x16x32_bf16 %0, v[112:115], v[148:151], %0\n\t" \
  "s_waitcnt lgkmcnt(10)\n\t" \
  "v_mfma_f32_16x16x32_bf16 %3, v[132:135], v[152:155], %3\n\t" \
  "ds_read_b64 v[144:145], %17 offset:512\n\t" \
  "v_mfma_f32_16x16x32_bf16 %2, v[124:127], v[152:155], %2\n\t" \
  "ds_read_b64 v[146:147], %17 offset:520\n\t" \
  "v_mfma_f32_16x16x32_bf16 %1, v[116:119], v[152:155], %1\n\t" \
  "v_mfma_f32_16x16x32_bf16 %0, v[108:111], v[152:155], %0\n\t" \
  "s_waitcnt lgkmcnt(10)\n\t" \
  "v_mfma_f32_16x16x32_bf16 %3, v[128:131], v[156:159], %3\n\t" \
  "ds_read_b64 v[148:149], %17 offset:576\n\t" \
  "v_mfma_f32_16x16x32_bf16 %2, v[120:123], v[156:159], %2\n\t" \
  "ds_read_b64 v[150:151], %17 offset:584\n\t" \
  "v_mfma_f32_16x16x32_bf16 %1, v[112:115], v[156:159], %1\n\t" \
  "v_mfma_f32_16x16x32_bf16 %0, v[104:107], v[156:159], %0\n\t" \
  "s_waitcnt lgkmcnt(10)\n\t" \
  "v_mfma_f32_16x16x32_bf16 %3, v[124:127], v[160:163], %3\n\t" \
  "ds_read_b64 v[152:153], %17 offset:640\n\t" \
  "v_mfma_f32_16x16x32_bf16 %2, v[116:119], v[160:163], %2\n\t" \
  "ds_read_b64 v[154:155], %17 offset:648\n\t" \
  "v_mfma_f32_16x16x32_bf16 %1, v[108:111], v[160:163], %1\n\t" \
  "v_mfma_f32_16x16x32_bf16 %0, v[100:103], v[160:163], %0\n\t" \
  "s_waitcnt lgkmcnt(10)\n\t" \
  "v_mfma_f32_16x16x32_bf16 %3, v[120:123], v[164:167], %3\n\t" \
  "ds_read_b64 v[156:157], %17 offset:704\n\t" \
  "v_mfma_f32_16x16x32_bf16 %2, v[112:115], v[164:167], %2\n\t" \
  "ds_read_b64 v[158:159], %17 offset:712\n\t" \
  "v_mfma_f32_16x16x32_bf16 %1, v[104:107], v[164:167], %1\n\t" \
  "v_mfma_f32_16x16x32_bf16 %0, v[96:99], v[164:167], %0\n\t" \
  "s_waitcnt lgkmcnt(10)\n\t" \
  "v_mfma_f32_16x16x32_bf16 %3, v[116:119], v[168:171], %3\n\t" \
  "ds_read_b64 v[160:161], %17 offset:1168\n\t" \
  "v_mfma_f32_16x16x32_bf16 %2, v[108:111], v[168:171], %2\n\t" \
  "ds_read_b64 v[162:163], %17 offset:1176\n\t" \
  "v_mfma_f32_16x16x32_bf16 %1, v[100:103], v[168:171], %1\n\t" \
  "s_waitcnt lgkmcnt(10)\n\t" \
  "v_mfma_f32_16x16x32_bf16 %3, v[112:115], v[172:175], %3\n\t" \
  "ds_read_b64 v[164:165], %17 offset:1232\n\t" \
  "v_mfma_f32_16x16x32_bf16 %2, v[104:107], v[172:175], %2\n\t" \
  "ds_read_b64 v[166:167], %17 offset:1240\n\t" \
  "v_mfma_f32_16x16x32_bf16 %1, v[96:99], v[172:175], %1\n\t" \
  "s_waitcnt lgkmcnt(10)\n\t" \
  "v_mfma_f32_16x16x32_bf16 %3, v[108:111], v[144:147], %3\n\t" \
  "ds_read_b64 v[168:169], %17 offset:1296\n\t" \
  "v_mfma_f32_16x16x32_bf16 %2, v[100:103], v[144:147], %2\n\t" \
  "ds_read_b64 v[170:171], %17 offset:1304\n\t" \
  "s_waitcnt lgkmcnt(10)\n\t" \
  "v_mfma_f32_16x16x32_bf16 %3, v[104:107], v[148:151], %3\n\t" \
  "ds_read_b64 v[172:173], %17 offset:1360\n\t" \
  "v_mfma_f32_16x16x32_bf16 %2, v[96:99], v[148:151], %2\n\t" \
  "ds_read_b64 v[174:175], %17 offset:1368\n\t" \
  "s_waitcnt lgkmcnt(10)\n\t" \
  "v_mfma_f32_16x16x32_bf16 %3, v[100:103], v[152:155], %3\n\t" \
  "ds_read_b64 v[144:145], %17 offset:1424\n\t" \
  "ds_read_b64 v[146:147], %17 offset:1432\n\t" \
  "s_waitcnt lgkmcnt(10)\n\t" \
  "v_mfma_f32_16x16x32_bf16 %3, v[96:99], v[156:159], %3\n\t" \
  "ds_read_b64 v[148:149], %17 offset:1488\n\t" \
  "ds_read_b64 v[150:151], %17 offset:1496\n\t" \
  "s_waitcnt vmcnt(0)\n\t" \
  "s_mov_b64 exec, 1\n\t" \
  "ds_max_u32 %18, %19\n\t" \
  "s_mov_b64 exec, -1\n\t" \
  "s_waitcnt lgkmcnt(10)\n\t" \
  "v_mfma_f32_16x16x32_bf16 %7, v[140:143], v[160:163], 0\n\t" \
  "ds_read_b64 v[152:153], %17 offset:1552\n\t" \
  "v_mfma_f32_16x16x32_bf16 %6, v[132:135], v[160:163], 0\n\t" \
  "ds_read_b64 v[154:155], %17 offset:1560\n\t" \
  "v_mfma_f32_16x16x32_bf16 %5, v[124:127], v[160:163], 0\n\t" \
  "v_mfma_f32_16x16x32_bf16 %4, v[116:119], v[160:163], 0\n\t" \
  "s_waitcnt lgkmcnt(10)\n\t" \
  "v_mfma_f32_16x16x32_bf16 %7, v[136:139], v[164:167], %7\n\t" \
  "ds_read_b64 v[156:157], %17 offset:1616\n\t" \
  "v_mfma_f32_16x16x32_bf16 %6, v[128:131], v[164:167], %6\n\t" \
  "ds_read_b64 v[158:159], %17 offset:1624\n\t" \
  "v_mfma_f32_16x16x32_bf16 %5, v[120:123], v[164:167], %5\n\t" \
  "v_mfma_f32_16x16x32_bf16 %4, v[112:115], v[164:167], %4\n\t" \
  "s_waitcnt lgkmcnt(10)\n\t" \
  "v_mfma_f32_16x16x32_bf16 %7, v[132:135], v[168:171], %7\n\t" \
  "ds_read_b64 v[160:161], %17 offset:1680\n\t" \
  "v_mfma_f32_16x16x32_bf16 %6, v[124:127], v[168:171], %6\n\t" \
  "ds_read_b64 v[162:163], %17 offset:1688\n\t" \
  "v_mfma_f32_16x16x32_bf16 %5, v[116:119], v[168:171], %5\n\t" \
  "v_mfma_f32_16x16x32_bf16 %4, v[108:111], v[168:171], %4\n\t" \
  "s_waitcnt lgkmcnt(10)\n\t" \
  "v_mfma_f32_16x16x32_bf16 %7, v[128:131], v[172:175], %7\n\t" \
  "ds_read_b64 v[164:165], %17 offset:1744\n\t" \
  "v_mfma_f32_16x16x32_bf16 %6, v[120:123], v[172:175], %6\n\t" \
  "ds_read_b64 v[166:167], %17 offset:1752\n\t" \
  "v_mfma_f32_16x16x32_bf16 %5, v[112:115], v[172:175], %5\n\t" \
  "v_mfma_f32_16x16x32_bf16 %4, v[104:107], v[172:175], %4\n\t" \
  "s_waitcnt lgkmcnt(10)\n\t" \
  "v_mfma_f32_16x16x32_bf16 %7, v[124:127], v[144:147], %7\n\t" \
  "ds_read_b64 v[168:169], %17 offset:1808\n\t" \
  "v_mfma_f32_16x16x32_bf16 %6, v[116:119], v[144:147], %6\n\t" \
  "ds_read_b64 v[170:171], %17 offset:1816\n\t" \
  "v_mfma_f32_16x16x32_bf16 %5, v[108:111], v[144:147], %5\n\t" \
  "v_mfma_f32_16x16x32_bf16 %4, v[100:103], v[144:147], %4\n\t" \
  "s_waitcnt lgkmcnt(10)\n\t" \
  "v_mfma_f32_16x16x32_bf16 %7, v[120:123], v[148:151], %7\n\t" \
  "ds_read_b64 v[172:173], %17 offset:1872\n\t" \
  "v_mfma_f32_16x16x32_bf16 %6, v[112:115], v[148:151], %6\n\t" \
  "ds_read_b64 v[174:175], %17 offset:1880\n\t" \
  "v_mfma_f32_16x16x32_bf16 %5, v[104:107], v[148:151], %5\n\t" \
  "v_mfma_f32_16x16x32_bf16 %4, v[96:99], v[148:151], %4\n\t" \
  "s_waitcnt lgkmcnt(10)\n\t" \
  "v_mfma_f32_16x16x32_bf16 %7, v[116:119], v[152:155], %7\n\t" \
  "ds_read_b64 v[144:145], %17 offset:2336\n\t" \
  "v_mfma_f32_16x16x32_bf16 %6, v[108:111], v[152:155], %6\n\t" \
  "ds_read_b64 v[146:147], %17 offset:2344\n\t" \
  "v_mfma_f32_16x16x32_bf16 %5, v[100:103], v[152:155], %5\n\t" \
  "s_waitcnt lgkmcnt(10)\n\t" \
  "v_mfma_f32_16x16x32_bf16 %7, v[112:115], v[156:159], %7\n\t" \
  "ds_read_b64 v[148:149], %17 offset:2400\n\t" \
  "v_mfma_f32_16x16x32_bf16 %6, v[104:107], v[156:159], %6\n\t" \
  "ds_read_b64 v[150:151], %17 offset:2408\n\t" \
  "v_mfma_f32_16x16x32_bf16 %5, v[96:99], v[156:159], %5\n\t" \
  "s_waitcnt lgkmcnt(10)\n\t" \
  "v_mfma_f32_16x16x32_bf16 %7, v[108:111], v[160:163], %7\n\t" \
  "ds_read_b64 v[152:153], %17 offset:2464\n\t" \
  "v_mfma_f32_16x16x32_bf16 %6, v[100:103], v[160:163], %6\n\t" \
  "ds_read_b64 v[154:155], %17 offset:2472\n\t" \
  "s_waitcnt lgkmcnt(10)\n\t" \
  "v_mfma_f32_16x16x32_bf16 %7, v[104:107], v[164:167], %7\n\t" \
  "ds_read_b64 v[156:157], %17 offset:2528\n\t" \
  "v_mfma_f32_16x16x32_bf16 %6, v[96:99], v[164:167], %6\n\t" \
  "ds_read_b64 v[158:159], %17 offset:2536\n\t" \
  "s_waitcnt lgkmcnt(10)\n\t" \
  "v_mfma_f32_16x16x32_bf16 %7, v[100:103], v[168:171], %7\n\t" \
  "ds_read_b64 v[160:161], %17 offset:2592\n\t" \
  "ds_read_b64 v[162:163], %17 offset:2600\n\t" \
  "s_waitcnt lgkmcnt(10)\n\t" \
  "v_mfma_f32_16x16x32_bf16 %7, v[96:99], v[172:175], %7\n\t" \
  "ds_read_b64 v[164:165], %17 offset:2656\n\t" \
  "ds_read_b64 v[166:167], %17 offset:2664\n\t" \
  "s_waitcnt lgkmcnt(10)\n\t" \
  "v_mfma_f32_16x16x32_bf16 %11, v[140:143], v[144:147], 0\n\t" \
  "ds_read_b64 v[168:169], %17 offset:2720\n\t" \
  "v_mfma_f32_16x16x32_bf16 %10, v[132:135], v[144:147], 0\n\t" \
  "ds_read_b64 v[170:171], %17 offset:2728\n\t" \
  "v_mfma_f32_16x16x32_bf16 %9, v[124:127], v[144:147], 0\n\t" \
  "v_mfma_f32_16x16x32_bf16 %8, v[116:119], v[144:147], 0\n\t" \
  "s_waitcnt lgkmcnt(10)\n\t" \
  "v_mfma_f32_16x16x32_bf16 %11, v[136:139], v[148:151], %11\n\t" \
  "ds_read_b64 v[172:173], %17 offset:2784\n\t" \
  "v_mfma_f32_16x16x32_bf16 %10, v[128:131], v[148:151], %10\n\t" \
  "ds_read_b64 v[174:175], %17 offset:2792\n\t" \
  "v_mfma_f32_16x16x32_bf16 %9, v[120:123], v[148:151], %9\n\t" \
  "v_mfma_f32_16x16x32_bf16 %8, v[112:115], v[148:151], %8\n\t" \
  "s_waitcnt lgkmcnt(10)\n\t" \
  "v_mfma_f32_16x16x32_bf16 %11, v[132:135], v[152:155], %11\n\t" \
  "ds_read_b64 v[144:145], %17 offset:2848\n\t" \
  "v_mfma_f32_16x16x32_bf16 %10, v[124:127], v[152:155], %10\n\t" \
  "ds_read_b64 v[146:147], %17 offset:2856\n\t" \
  "v_mfma_f32_16x16x32_bf16 %9, v[116:119], v[152:155], %9\n\t" \
  "v_mfma_f32_16x16x32_bf16 %8, v[108:111], v[152:155], %8\n\t" \
  "s_waitcnt lgkmcnt(10)\n\t" \
  "v_mfma_f32_16x16x32_bf16 %11, v[128:131], v[156:159], %11\n\t" \
  "ds_read_b64 v[148:149], %17 offset:2912\n\t" \
  "v_mfma_f32_16x16x32_bf16 %10, v[120:123], v[156:159], %10\n\t" \
  "ds_read_b64 v[150:151], %17 offset:2920\n\t" \
  "v_mfma_f32_16x16x32_bf16 %9, v[112:115], v[156:159], %9\n\t" \
  "v_mfma_f32_16x16x32_bf16 %8, v[104:107], v[156:159], %8\n\t" \
  "s_waitcnt lgkmcnt(10)\n\t" \
  "v_mfma_f32_16x16x32_bf16 %11, v[124:127], v[160:163], %11\n\t" \
  "ds_read_b64 v[152:153], %17 offset:2976\n\t" \
  "v_mfma_f32_16x16x32_bf16 %10, v[116:119], v[160:163], %10\n\t" \
  "ds_read_b64 v[154:155], %17 offset:2984\n\t" \
  "v_mfma_f32_16x16x32_bf16 %9, v[108:111], v[160:163], %9\n\t" \
  "v_mfma_f32_16x16x32_bf16 %8, v[100:103], v[160:163], %8\n\t" \
  "s_waitcnt lgkmcnt(10)\n\t" \
  "v_mfma_f32_16x16x32_bf16 %11, v[120:123], v[164:167], %11\n\t" \
  "ds_read_b64 v[156:157], %17 offset:3040\n\t" \
  "v_mfma_f32_16x16x32_bf16 %10, v[112:115], v[164:167], %10\n\t" \
  "ds_read_b64 v[158:159], %17 offset:3048\n\t" \
  "v_mfma_f32_16x16x32_bf16 %9, v[104:107], v[164:167], %9\n\t" \
  "v_mfma_f32_16x16x32_bf16 %8, v[96:99], v[164:167], %8\n\t" \
  "s_waitcnt lgkmcnt(10)\n\t" \
  "v_mfma_f32_16x16x32_bf16 %11, v[116:119], v[168:171], %11\n\t" \
  "ds_read_b64 v[160:161], %17 offset:3504\n\t" \
  "v_mfma_f32_16x16x32_bf16 %10, v[108:111], v[168:171], %10\n\t" \
  "ds_read_b64 v[162:163], %17 offset:3512\n\t" \
  "v_mfma_f32_16x16x32_bf16 %9, v[100:103], v[168:171], %9\n\t" \
  "s_waitcnt lgkmcnt(10)\n\t" \
  "v_mfma_f32_16x16x32_bf16 %11, v[112:115], v[172:175], %11\n\t" \
  "ds_read_b64 v[164:165], %17 offset:3568\n\t" \
  "v_mfma_f32_16x16x32_bf16 %10, v[104:107], v[172:175], %10\n\t" \
  "ds_read_b64 v[166:167], %17 offset:3576\n\t" \
  "v_mfma_f32_16x16x32_bf16 %9, v[96:99], v[172:175], %9\n\t" \
  "s_waitcnt lgkmcnt(10)\n\t" \
  "v_mfma_f32_16x16x32_bf16 %11, v[108:111], v[144:147], %11\n\t" \
  "ds_read_b64 v[168:169], %17 offset:3632\n\t" \
  "v_mfma_f32_16x16x32_bf16 %10, v[100:103], v[144:147], %10\n\t" \
  "ds_read_b64 v[170:171], %17 offset:3640\n\t" \
  "s_waitcnt lgkmcnt(10)\n\t" \
  "v_mfma_f32_16x16x32_bf16 %11, v[104:107], v[148:151], %11\n\t" \
  "ds_read_b64 v[172:173], %17 offset:3696\n\t" \
  "v_mfma_f32_16x16x32_bf16 %10, v[96:99], v[148:151], %10\n\t" \
  "ds_read_b64 v[174:175], %17 offset:3704\n\t" \
  "s_waitcnt lgkmcnt(10)\n\t" \
  "v_mfma_f32_16x16x32_bf16 %11, v[100:103], v[152:155], %11\n\t" \
  "ds_read_b64 v[144:145], %17 offset:3760\n\t" \
  "ds_read_b64 v[146:147], %17 offset:3768\n\t" \
  "s_waitcnt lgkmcnt(10)\n\t" \
  "v_mfma_f32_16x16x32_bf16 %11, v[96:99], v[156:159], %11\n\t" \
  "ds_read_b64 v[148:149], %17 offset:3824\n\t" \
  "ds_read_b64 v[150:151], %17 offset:3832\n\t" \
  "s_waitcnt lgkmcnt(10)\n\t" \
  "v_mfma_f32_16x16x32_bf16 %15, v[140:143], v[160:163], 0\n\t" \
  "ds_read_b64 v[152:153], %17 offset:3888\n\t" \
  "v_mfma_f32_16x16x32_bf16 %14, v[132:135], v[160:163], 0\n\t" \
  "ds_read_b64 v[154:155], %17 offset:3896\n\t" \
  "v_mfma_f32_16x16x32_bf16 %13, v[124:127], v[160:163], 0\n\t" \
  "v_mfma_f32_16x16x32_bf16 %12, v[116:119], v[160:163], 0\n\t" \
  "s_waitcnt lgkmcnt(10)\n\t" \
  "v_mfma_f32_16x16x32_bf16 %15, v[136:139], v[164:167], %15\n\t" \
  "ds_read_b64 v[156:157], %17 offset:3952\n\t" \
  "v_mfma_f32_16x16x32_bf16 %14, v[128:131], v[164:167], %14\n\t" \
  "ds_read_b64 v[158:159], %17 offset:3960\n\t" \
  "v_mfma_f32_16x16x32_bf16 %13, v[120:123], v[164:167], %13\n\t" \
  "v_mfma_f32_16x16x32_bf16 %12, v[112:115], v[164:167], %12\n\t" \
  "s_waitcnt lgkmcnt(10)\n\t" \
  "v_mfma_f32_16x16x32_bf16 %15, v[132:135], v[168:171], %15\n\t" \
  "ds_read_b64 v[160:161], %17 offset:4016\n\t" \
  "v_mfma_f32_16x16x32_bf16 %14, v[124:127], v[168:171], %14\n\t" \
  "ds_read_b64 v[162:163], %17 offset:4024\n\t" \
  "v_mfma_f32_16x16x32_bf16 %13, v[116:119], v[168:171], %13\n\t" \
  "v_mfma_f32_16x16x32_bf16 %12, v[108:111], v[168:171], %12\n\t" \
  "s_waitcnt lgkmcnt(10)\n\t" \
  "v_mfma_f32_16x16x32_bf16 %15, v[128:131], v[172:175], %15\n\t" \
  "ds_read_b64 v[164:165], %17 offset:4080\n\t" \
  "v_mfma_f32_16x16x32_bf16 %14, v[120:123], v[172:175], %14\n\t" \
  "ds_read_b64 v[166:167], %17 offset:4088\n\t" \
  "v_mfma_f32_16x16x32_bf16 %13, v[112:115], v[172:175], %13\n\t" \
  "v_mfma_f32_16x16x32_bf16 %12, v[104:107], v[172:175], %12\n\t" \
  "s_waitcnt lgkmcnt(10)\n\t" \
  "v_mfma_f32_16x16x32_bf16 %15, v[124:127], v[144:147], %15\n\t" \
  "ds_read_b64 v[168:169], %17 offset:4144\n\t" \
  "v_mfma_f32_16x16x32_bf16 %14, v[116:119], v[144:147], %14\n\t" \
  "ds_read_b64 v[170:171], %17 offset:4152\n\t" \
  "v_mfma_f32_16x16x32_bf16 %13, v[108:111], v[144:147], %13\n\t" \
  "v_mfma_f32_16x16x32_bf16 %12, v[100:103], v[144:147], %12\n\t" \
  "s_waitcnt lgkmcnt(10)\n\t" \
  "v_mfma_f32_16x16x32_bf16 %15, v[120:123], v[148:151], %15\n\t" \
  "ds_read_b64 v[172:173], %17 offset:4208\n\t" \
  "v_mfma_f32_16x16x32_bf16 %14, v[112:115], v[148:151], %14\n\t" \
  "ds_read_b64 v[174:175], %17 offset:4216\n\t" \
  "v_mfma_f32_16x16x32_bf16 %13, v[104:107], v[148:151], %13\n\t" \
  "v_mfma_f32_16x16x32_bf16 %12, v[96:99], v[148:151], %12\n\t" \
  "s_waitcnt lgkmcnt(10)\n\t" \
  "v_mfma_f32_16x16x32_bf16 %15, v[116:119], v[152:155], %15\n\t" \
  "v_mfma_f32_16x16x32_bf16 %14, v[108:111], v[152:155], %14\n\t" \
  "v_mfma_f32_16x16x32_bf16 %13, v[100:103], v[152:155], %13\n\t" \
  "s_waitcnt lgkmcnt(8)\n\t" \
  "v_mfma_f32_16x16x32_bf16 %15, v[112:115], v[156:159], %15\n\t" \
  "v_mfma_f32_16x16x32_bf16 %14, v[104:107], v[156:159], %14\n\t" \
  "v_mfma_f32_16x16x32_bf16 %13, v[96:99], v[156:159], %13\n\t" \
  "s_waitcnt lgkmcnt(6)\n\t" \
  "v_mfma_f32_16x16x32_bf16 %15, v[108:111], v[160:163], %15\n\t" \
  "v_mfma_f32_16x16x32_bf16 %14, v[100:103], v[160:163], %14\n\t" \
  "s_waitcnt lgkmcnt(4)\n\t" \
  "v_mfma_f32_16x16x32_bf16 %15, v[104:107], v[164:167], %15\n\t" \
  "v_mfma_f32_16x16x32_bf16 %14, v[96:99], v[164:167], %14\n\t" \
  "s_waitcnt lgkmcnt(2)\n\t" \
  "v_mfma_f32_16x16x32_bf16 %15, v[100:103], v[168:171], %15\n\t" \
  "s_waitcnt lgkmcnt(0)\n\t" \
  "v_mfma_f32_16x16x32_bf16 %15, v[96:99], v[172:175], %15\n\t" \
  "s_nop 7\n\t"
#define CVB_CLOB "v96", "v97", "v98", "v99", "v100", "v101", "v102", "v103", "v104", "v105", "v106", "v107", "v108", "v109", "v110", "v111", "v112", "v113", "v114", "v115", "v116", "v117", "v118", "v119", "v120", "v121", "v122", "v123", "v124", "v125", "v126", "v127", "v128", "v129", "v130", "v131", "v132", "v133", "v134", "v135", "v136", "v137", "v138", "v139", "v140", "v141", "v142", "v143", "v144", "v145", "v146", "v147", "v148", "v149", "v150", "v151", "v152", "v153", "v154", "v155", "v156", "v157", "v158", "v159", "v160", "v161", "v162", "v163", "v164", "v165", "v166", "v167", "v168", "v169", "v170", "v171", "v172", "v173", "v174", "v175"

typedef unsigned v16u __attribute__((ext_vector_type(16)));
typedef unsigned v8u __attribute__((ext_vector_type(8)));
DEVI char* mkptr(unsigned lo, unsigned hi) { return (char*)(((unsigned long long)hi << 32) | lo); }

NOPK __global__ __launch_bounds__(512, 2) void fused_kernel(
    const u16* a0, const u16* a1, const u16* a2, const u16* a3, const u16* a4, const u16* a5, const u16* a6, const u16* a7,
    const u16* a8, const u16* a9, u16* a10, char* a11) {
  v16u ka; v8u kb8;
  asm volatile("s_load_dwordx16 %0, %2, 0x0\n\ts_load_dwordx8 %1, %2, 0x40" : "=&s"(ka), "=&s"(kb8) : "s"(__builtin_amdgcn_kernarg_segment_ptr()));
  __shared__ __attribute__((aligned(16))) char smem[LDS_BYTES];
  LAS char* const sm = (LAS char*)smem;
  const unsigned lb = (unsigned)(uintptr_t)sm;
  const int bid = __builtin_amdgcn_workgroup_id_x(), tid = __builtin_amdgcn_workitem_id_x(), lane = tid & 63;
  const int w = __builtin_amdgcn_readfirstlane(tid >> 6);
  const int xcd = bid & 7, idx = bid >> 3;
  unsigned pfc = 0;
  {
    unsigned long long kpc;
    asm volatile("s_getpc_b64 %0" : "=s"(kpc));
    const unsigned o = imin((((idx & 3) * 8 + w) * 8 + (lane & 7)) * 128, CODE_BYTES);
    asm volatile("s_mov_b64 exec, 0xff\n\tglobal_load_dword %0, %1, %2\n\ts_mov_b64 exec, -1" : "+v"(pfc) : "v"(o), "s"(kpc) : "memory");
  }
  const bool idle1 = idx >= 30;
  const int i7 = (idx * 37) >> 8;
  const int rb = (xcd >> 1) * 4 + (idx < 28 ? i7 : (xcd & 1) * 2 + (idx & 1));
  const int cbx = imin((xcd & 1) * 8 + (xcd >> 1) * 2 + (idx & 1), 14), cby = (xcd & 1) * 8 + idx - 7 * i7;
  const int cb = idx < 28 ? cby : (idx < 30 ? 7 : cbx);
  const int col0 = imin(cb * 96, 1280);
  const int cp = xcd * 32 + idx;
  const int tm = xcd * 4 + (idx >> 3), tn = idx & 7;
  const int hh = lane >> 5, l31 = lane & 31, l15 = lane & 15, g4 = lane >> 4;
  unsigned xv[8], gv[3], uv[3];
#pragma unroll
  for (int j = 0; j < 8; ++j) { xv[j] = (lane >> 3) * 1024 + (((lane & 7) ^ ((4 * j + g4) & 7)) << 4) + j * 7168 + 4096; PIN(xv[j]); }
  if (tid < 24) *(LAS volatile unsigned*)(sm + L_SYNC + tid * 4) = 0u;
  asm volatile("s_waitcnt lgkmcnt(0)\n\ts_barrier" : "+s"(ka), "+s"(kb8) :: "memory");
  const char* const x = mkptr(ka[0], ka[1]);
  const char* const wg = mkptr(ka[14], ka[15]);
  const char* const wu = mkptr(kb8[0], kb8[1]);
  const char* const wd = mkptr(kb8[2], kb8[3]);
  char* const out = mkptr(kb8[4], kb8[5]);
  char* const ws = mkptr(kb8[6], kb8[7]);
  const char* xb = x + rb * 131072;
  unsigned slot = lb + SLOT;
  const int kstep = idle1 ? -64 * 2752 : 64 * 2752;
  const char* gb = wg + col0 * 2 + (idle1 ? 448 * 2752 : 0);
  const char* ub = wu + col0 * 2 + (idle1 ? 448 * 2752 : 0);
#define LD_W(k) glds6(gv, uv, gb + (k) * (16 * 2752), ub + (k) * (16 * 2752), slot + 16384 + 3072 + (k) * 6144)
#define LD_NEXT() do { xb += 128; gb += kstep; ub += kstep; slot = slot == lb + 2 * SLOT ? lb : slot + SLOT; } while (0)
  if (w == 7 && !idle1) { glds8(xv, xb, slot + 4096); glds8(xv, xb + 65536, slot + 12288); }
  v4u e0 = {0u, 0u, 0u, 0u}, e1 = {0u, 0u, 0u, 0u};
  unsigned pfw = 0;
  if (w < 4) {
    const char* xr = x + (cp * 8 + 2 * w) * 1024;
    asm volatile("global_load_dwordx4 %0, %2, %3\n\tglobal_load_dwordx4 %1, %2, %3 offset:512"
                 : "+v"(e0), "+v"(e1) : "v"(hh * 1024 + l31 * 16), "s"(xr) : "memory");
  } else if (w < 6) {
    unsigned q0 = 0, q1 = 0;
    asm volatile("global_load_dword %0, %2, %3\n\tglobal_load_dword %1, %2, %4" : "+v"(q0), "+v"(q1) : "v"(cp * 4), "s"(mkptr(ka[10], ka[11])), "s"(mkptr(ka[12], ka[13])) : "memory");
    e1.x = q0; e1.y = q1;
    const unsigned po = (2 * cp + w - 4) * 256 + lane * 4;
    unsigned p0 = 0, p1 = 0, p2 = 0, p3 = 0;
    asm volatile("global_load_dword %0, %4, %5" PNT "\n\tglobal_load_dword %1, %4, %6" PNT "\n\tglobal_load_dword %2, %4, %7" PNT "\n\tglobal_load_dword %3, %4, %8" PNT
                 : "+v"(p0), "+v"(p1), "+v"(p2), "+v"(p3)
                 : "v"(po), "s"(mkptr(ka[2], ka[3])), "s"(mkptr(ka[4], ka[5])), "s"(mkptr(ka[6], ka[7])), "s"(mkptr(ka[8], ka[9])) : "memory");
    e0 = v4u{p0, p1, p2, p3};
  } else if (w == 6) {
    const int pl = imin(lane, 35), pr = (pl * 43) >> 9;
    const int prow = 128 + (xcd >> 1) * 96 + idx * 3 + pr;
    const unsigned po = prow * 2752 + (xcd & 1) * 1376 + imin((pl - 12 * pr) * 128, 1372);
    unsigned g0 = 0, g1 = 0;
    asm volatile("global_load_dword %0, %3, %4 sc1\n\tglobal_load_dword %1, %5, %6\n\tglobal_load_dword %2, %5, %7"
                 : "+v"(pfw), "+v"(g0), "+v"(g1) : "v"(0u), "s"(ws + WS_E), "v"(po), "s"(wg), "s"(wu) : "memory");
    e0.x = g0; e0.y = g1;
  }

  if (w == 7) {
    __builtin_amdgcn_s_setprio(LPRIO);
    const char* dbase = (const char*)wd + tn * 128;
    const unsigned fa = lb + S_FULL;
    unsigned fcnt = 0;
    {
      v4u fr;
#pragma unroll
      for (int j = 0; j < 3; ++j) {
        int p = 16 * j + (lane >> 2);
        int o = (p / 3) * 2752 + (p % 3) * 64 + (lane & 3) * 16 - j * 1024;
        gv[j] = o + 3072; uv[j] = o ^ 32;
      }
      LD_W(0); LD_W(1); LD_W(2); LD_W(3); LD_NEXT();
      if (idle1) {
#pragma unroll 1
        for (int s = 1; s < 8; ++s) {
          if (s >= 4) wait_free(sm, s - 3);
          slot = lb + 24576 * ((s + 2) & 3) - 8192;
          LD_W(0); LD_W(1); LD_W(2); LD_W(3);
          gb += kstep; ub += kstep;
          if (s >= 2) { fcnt = 4 * s - 5; publish<CSLAG>(fa, fcnt); }
        }
        fcnt = 27; publish<24>(fa, fcnt);
        fcnt = 31; publish<0>(fa, fcnt); fcnt = 0;
        wait_free(sm, 8);
      } else {
      glds8(xv, xb, slot + 4096); publish<26>(fa, fcnt);
      glds8(xv, xb + 65536, slot + 12288); publish<28>(fa, fcnt);
      LD_W(0); publish<28>(fa, fcnt);
      LD_W(1); publish<28>(fa, fcnt);
      LD_W(2); LD_W(3); LD_NEXT();
#pragma unroll 1
      for (int s = 2; s < 8; ++s) {
        glds8(xv, xb, slot + 4096); glds8(xv, xb + 65536, slot + 12288); publish<34>(fa, fcnt);
        LD_W(0); publish<34>(fa, fcnt);
        LD_W(1); publish<34>(fa, fcnt);
        LD_W(2); publish<34>(fa, fcnt);
        fr = *(LAS volatile v4u*)(sm + S_FREE);
        LD_W(3);
        LD_NEXT();
        unsigned m_ = fr.x < fr.y ? fr.x : fr.y, m2_ = fr.z < fr.w ? fr.z : fr.w;
        if (__builtin_expect((int)__builtin_amdgcn_readfirstlane(m_ < m2_ ? m_ : m2_) < s - 1, 0)) {
#pragma unroll 1
          for (unsigned i = 0; i < LSPIN_CAP && rd_free(sm) < s - 1; ++i) {}
        }
      }
      }
    }
    const unsigned d0 = (lane >> 3) * 1024 + (((lane & 7) ^ ((g4 & 1) << 2)) << 4), d1 = d0 + 7168;
    const char* dhi = dbase + 43 * 16384 - 2048;
#define LD_P(n, ldsa) gldsp(d0, d1, d0, d1, dbase + (n) * 16384, dhi + (n) * 16384, (ldsa))
    PIN(pfc);
    LD_P(0, lb + L_WD); LD_P(1, lb + L_WD + 4096);
    publish<26>(fa, fcnt);
    LD_P(2, lb + L_WD + 2 * 4096); publish<24>(fa, fcnt);
    LD_P(3, lb + L_WD + 3 * 4096); publish<22>(fa, fcnt);
    LD_P(4, lb + L_WD + 4 * 4096); publish<20>(fa, fcnt);
    __builtin_amdgcn_s_setprio(LPRIO2);
#pragma unroll 1
    for (int n = 5; n < 10; ++n) LD_P(n, lb + L_WD + n * 4096);
    wait_free(sm, 7);
#pragma unroll 1
    for (int n = 10; n < 20; ++n) LD_P(n, lb + L_WD + n * 4096);
    fcnt = 45; publish<24>(fa, fcnt);
    if ((lds_rd(sm, S_URDY) < 1) | (lds_rd(sm, S_KRDY) < 2)) { fcnt = 51; publish<0>(fa, fcnt); }
    __builtin_amdgcn_s_setprio(3);
  }
  unsigned tag;

  if (w < 4) {
    __builtin_amdgcn_s_setprio(3);
    const int tb2 = w & 1, chf = w >> 1;
    const unsigned fulladdr = lb + S_FULL;
    {
      WAITV(0); SCHED();
      asm volatile("" : "+v"(e0), "+v"(e1), "+v"(pfc) :: "memory");
      const v4u xq[2] = {e0, e1};
      float v[16];
#pragma unroll
      for (int j = 0; j < 8; ++j) { v[2 * j] = bflo(xq[j >> 2][j & 3]); v[2 * j + 1] = bfhi(xq[j >> 2][j & 3]); }
      float s0 = (v[0] + v[4]) + (v[8] + v[12]), s1 = (v[1] + v[5]) + (v[9] + v[13]), s2 = (v[2] + v[6]) + (v[10] + v[14]), s3 = (v[3] + v[7]) + (v[11] + v[15]);
      const float mean = sum32((s0 + s1) + (s2 + s3)) * (1.0f / 512);
      const float m = rbf(mean);
      s0 = s1 = 0.f;
#pragma unroll
      for (int i = 0; i < 16; i += 2) { v[i] -= m; v[i + 1] -= m; s0 += v[i] * v[i]; s1 += v[i + 1] * v[i + 1]; }
      const float dm = mean - m;
      const float var = rbf(relu(sum32(s0 + s1) * (1.0f / 512) - dm * dm));
      const float sd = rbf(__builtin_amdgcn_sqrtf(rbf(var + 1e-5f)));
      const float rs = __builtin_amdgcn_rcpf(sd);
      const int lw = L_U + (2 * w + hh) * LNP + l31 * 16;
#pragma unroll
      for (int i = 0; i < 2; ++i) {
        v4u o;
#pragma unroll
        for (int j = 0; j < 4; ++j) { unsigned d = pkbf(v[8 * i + 2 * j], v[8 * i + 2 * j + 1]); o[j] = pkbf(bflo(d) * rs, bfhi(d) * rs); }
        ST128(lw + i * 512, o);
      }
      if (l31 == 0) *(LAS unsigned*)(sm + L_STW + (2 * w + hh) * 4) = pkbf(-m, sd);
      sig_add(sm, S_LNW, lane);
      __builtin_amdgcn_s_setprio(MPRIO);
    }
    unsigned xa[4], wa[3];
    {
      int tok = 64 * tb2 + l31;
      int b0 = lb + SLOT + tok * 128 + ((hh ^ ((tok >> 1) & 7)) << 4);
#pragma unroll
      for (int s = 0; s < 4; ++s) xa[s] = b0 ^ (s << 5);
      int mat = g4 & 1, q = (lane >> 2) & 3, p = lane & 3;
#pragma unroll
      for (int t = 0; t < 3; ++t) {
        int T = 3 * chf + t;
        wa[t] = lb + SLOT + 16384 + mat * 3072 + (8 * hh + q) * 192 + (T >> 1) * 64 + (((T & 1) ^ mat) << 5) + p * 8;
      }
    }
    if (idle1) {
      __builtin_amdgcn_s_setprio(3);
      f32x4 cs[2][3];
#pragma unroll
      for (int t = 0; t < 3; ++t) { cs[0][t] = cs[1][t] = f32x4{0.f, 0.f, 0.f, 0.f}; wa[t] += tb2 * 768 - lb; }
      wait_ge(sm, S_TAGOK, 1);
      const unsigned tg = lds_rd(sm, S_TAG);
      unsigned seen = 0;
#pragma unroll 1
      for (int s = 0; s < 8; ++s) {
        if (seen < 4u * s + 4) seen = spin_ge(fulladdr, 4 * s + 4);
        v2u f[4][3];
#pragma unroll
        for (int k = 0; k < 4; ++k)
#pragma unroll
          for (int t = 0; t < 3; ++t) f[k][t] = LDTR(wa[t] + k * 6144);
        sig_set(sm, S_FREE + w * 4, s + 1);
#pragma unroll
        for (int k = 0; k < 4; ++k)
#pragma unroll
          for (int t = 0; t < 3; ++t) cs[k & 1][t] = lanesum4(f[k][t].x, f[k][t].y, cs[k & 1][t]);
        const int adv = (s == 1 || s == 5) ? -73728 : 24576;
#pragma unroll
        for (int t = 0; t < 3; ++t) wa[t] += adv;
      }
#pragma unroll
      for (int t = 0; t < 3; ++t) *(LAS float*)(sm + L_CSX + ((w * 3 + t) * 64 + lane) * 4) = cs[0][t][0] + cs[1][t][0];
      if (add_ret(sm, S_CSF) == 3) {
        const LAS float* p = (const LAS float*)(sm + L_CSX) + 384 * hh + l31;
#pragma unroll
        for (int t = 0; t < 3; ++t) {
          float c = (p[64 * t] + p[64 * t + 32]) + (p[64 * t + 192] + p[64 * t + 224]);
          st8_sc1(v2u{__builtin_bit_cast(unsigned, c), tg}, (unsigned)((96 * hh + l31) * 8), ws + WS_CS + cb * 2048 + t * 256);
        }
      }
    } else if (cb == 14 && chf == 0) {
      sig_set(sm, S_FREE + w * 4, 8);
      wait_ge(sm, S_TAGOK, 1);
    } else {
      {
        unsigned fcnt = 0, need = 3, a0 = SLOT, a1 = (unsigned)(-2 * SLOT), a2 = SLOT, sf, st, lc = 6;
        char* hb = ws + WS_H + ((size_t)(4 * rb + 2 * tb2) * 86 + (col0 >> 4) + 3 * chf) * 1024;
        asm volatile(G1_ASM
                     : "+v"(xa[0]), "+v"(xa[1]), "+v"(xa[2]), "+v"(xa[3]), "+v"(wa[0]), "+v"(wa[1]), "+v"(wa[2]), "+v"(fcnt),
                       "+s"(need), "+s"(a0), "+s"(a1), "+s"(a2), "=&s"(sf), "=&s"(st), "+s"(lc)
                     : "v"(fulladdr), "v"(lb + S_FREE + w * 4), "v"(lb + S_CS), "v"(lb + L_CSX + (3 * chf * 32 + l31) * 8),
                       "v"(lb + L_NM + (64 * tb2 + l31) * 8), "v"(lane * 16), "s"(hb), "s"(hb + 86 * 1024), "s"(cb)
                     : "memory", "scc", "vcc", G1_CLOB);
      }
    }
    tag = lds_rd(sm, S_TAG);
    const int th = w & 1, kh = w >> 1;
    f32x16 oa[2];
#pragma unroll
    for (int e = 0; e < 2; ++e)
#pragma unroll
      for (int r = 0; r < 16; ++r) oa[e][r] = 0.f;
    {
      const int q = (lane >> 2) & 3, p = lane & 3;
      unsigned da0 = lb + L_WD + kh * 2048 + (4 * hh + q) * 128 + ((q >> 1) << 6) + (g4 & 1) * 32 + p * 8, da1 = da0 ^ 64;
      const char* hbase = ws + WS_H + ((size_t)(2 * tm + th) * 86 + 43 * kh) * 1024;
      asm volatile(G2_ASM
                   : "+v"(oa[0]), "+v"(oa[1]), "+v"(da0), "+v"(da1)
                   : "v"(lane * 16), "v"(lb + L_SYNC), "s"(hbase), "v"(imin(l15, 14) * F3S), "s"(ws + WS_F3 + (tm >> 1) * 4096), "s"(w), "s"(tag),
                     "v"(cb * F3S), "s"(ws + WS_F3 + rb * 4096), "s"(idle1 ? 99 : 3),
                     "v"((lane >> 3) * 1024 + (((lane & 7) ^ ((g4 & 1) << 2)) << 4)), "s"((const char*)wd + tn * 128 + (23 + 5 * w) * 16384),
                     "s"(lb + L_WD + (5 * w - 5) * 4096), "s"(w == 3 ? lb + L_KG : lb + L_WD + (5 * w - 1) * 4096), "s"(lb + L_WD + (20 + 2 * w) * 4096)
                   : "memory", "scc", "vcc", G2_CLOB);
    }
    wait_ge(sm, S_WDONE, 4);
#pragma unroll
    for (int e = 0; e < 2; ++e)
#pragma unroll
      for (int i = 0; i < 4; ++i)
        *(LAS f32x4*)(sm + L_RED + kh * REDQ + (32 * th + l31) * REDP + (32 * e + 8 * i + 4 * hh) * 4) =
            f32x4{oa[e][4 * i], oa[e][4 * i + 1], oa[e][4 * i + 2], oa[e][4 * i + 3]};
  } else {
    if (w < 6) {
      __builtin_amdgcn_s_setprio(XPRIO);
      const int ch = w - 4;
      WAITV(0); SCHED();
      asm volatile("" : "+v"(e0), "+v"(e1), "+v"(pfc) :: "memory");
      const unsigned ldt2 = e1.x, dsk2 = e1.y;
      const float dt = rbf(expacc(ch ? bfhi(ldt2) : bflo(ldt2)));
      const unsigned la = e0.x, ai = e0.y, cr = e0.z, ci = e0.w;
      int mx = imax(imax((int)(cr & 0x7fff), (int)((cr >> 16) & 0x7fff)), imax((int)(ci & 0x7fff), (int)((ci >> 16) & 0x7fff)));
      const int ex = imin(imax(wave_maxi(mx) >> 7, 1), 253);
      const float sc = __builtin_bit_cast(float, (unsigned)(254 - ex) << 23);
      const float kscale = 2.0f * dt * __builtin_bit_cast(float, (unsigned)ex << 23);
      float ar2[2], ai2[2]; cf c0[2];
      ar2[0] = -rbf(expacc(bflo(la))) * dt * 1.44269504088896341f; ar2[1] = -rbf(expacc(bfhi(la))) * dt * 1.44269504088896341f;
      ai2[0] = bflo(ai) * dt * 0.15915494309189535f; ai2[1] = bfhi(ai) * dt * 0.15915494309189535f;
      c0[0].r = bflo(cr) * sc; c0[0].i = bflo(ci) * sc; c0[1].r = bfhi(cr) * sc; c0[1].i = bfhi(ci) * sc;
      const int kb = L_KG + ch * KGCH, kw = kb + lane * 8, kr = kb + l15 * KGP + g4 * 16, kx = L_KX + ch * KXCH + 32;
      const int tb = L_TBL + ch * TBLCH;
      auto chain = [&](cf (&z)[2], const cf (&m)[2], int n, int row0, int rstep) NOPK __attribute__((always_inline)) {
#pragma unroll
        for (int i = 0; i < n; ++i) {
          ST64(kw + (row0 + i * rstep) * KGP, (v2u{pkh(z[0].r, z[0].i), pkh(z[1].r, z[1].i)}));
          z[0] = cmul(z[0], m[0]); z[1] = cmul(z[1], m[1]);
        }
      };
      cf m[2], m128[2], z[2], g[2];
      v4u yf[8];
      m[0] = cexpp(ar2[0], -ai2[0], 8.f); m[1] = cexpp(ar2[1], -ai2[1], 8.f);
      z[0].r = 1.f; z[0].i = 0.f; z[1] = z[0];
      chain(z, m, 16, 0, 1);
      MEMF();
#pragma unroll
      for (int s = 0; s < 8; ++s) yf[s] = LD128(kr + s * 64);
      MEMF();
      v4u xf[3][8];
#define KG_GRAB(t) do { MEMF(); _Pragma("unroll") for (int s = 0; s < 8; ++s) xf[t][s] = LD128(kr + s * 64); MEMF(); } while (0)
      m128[0] = cexpp(ar2[0], ai2[0], 128.f); m128[1] = cexpp(ar2[1], ai2[1], 128.f);
      m[0] = cexpp(ar2[0], ai2[0], 1.f); m[1] = cexpp(ar2[1], ai2[1], 1.f);
      z[0] = c0[0]; z[1] = c0[1];
      chain(z, m, 8, 0, 1);
      g[0] = cmul(c0[0], m128[0]); g[1] = cmul(c0[1], m128[1]); z[0] = g[0]; z[1] = g[1];
      chain(z, m, 8, 8, 1);
      KG_GRAB(0);
      m[0] = cmul(m[0], m[0]); m[1] = cmul(m[1], m[1]);
      g[0] = cmul(g[0], m128[0]); g[1] = cmul(g[1], m128[1]); z[0] = g[0]; z[1] = g[1];
      chain(z, m, 4, 0, 1);
      g[0] = cmul(g[0], m128[0]); g[1] = cmul(g[1], m128[1]); z[0] = g[0]; z[1] = g[1];
      chain(z, m, 4, 4, 1);
      m[0] = cmul(m[0], m[0]); m[1] = cmul(m[1], m[1]);
      g[0] = cmul(g[0], m128[0]); g[1] = cmul(g[1], m128[1]);
      z[0] = cmul(g[0], m[0]); z[1] = cmul(g[1], m[1]);
      chain(z, m128, 4, 9, 2);
      chain(g, m128, 4, 8, 2);
      KG_GRAB(1);
      chain(g, m128, 9, 0, 1);
      KG_GRAB(2);
      SCHED();
      __builtin_amdgcn_s_setprio(3);
      {
        f32x4 a[3];
#pragma unroll
        for (int t = 0; t < 3; ++t) a[t] = f32x4{0.f, 0.f, 0.f, 0.f};
#pragma unroll
        for (int s = 0; s < 8; ++s)
#pragma unroll
          for (int t = 0; t < 3; ++t) a[t] = mfma16h(xf[t][s], yf[s], a[t]);
        __builtin_amdgcn_s_setprio(XPRIO);
        const float dk = lane == 0 ? (ch ? bfhi(dsk2) : bflo(dsk2)) : 0.f;
        ST64(kx + 2 * (128 * (g4 >> 1) + 8 * l15 + 4 * (g4 & 1)), (v2u{pkbf(a[0][0] * kscale + dk, a[0][1] * kscale), pkbf(a[0][2] * kscale, a[0][3] * kscale)}));
        const int i0 = g4 < 2 ? 256 + 64 * g4 + 4 * l15 : 384 + 64 * (g4 - 2) + 2 * l15, i2 = g4 < 2 ? i0 + 2 : i0 + 32;
        *(LAS unsigned*)(sm + tb + 2 * i0) = pkbf(a[1][0] * kscale, a[1][1] * kscale);
        *(LAS unsigned*)(sm + tb + 2 * i2) = pkbf(a[1][2] * kscale, a[1][3] * kscale);
        const int i3 = tb + 2 * (512 + 64 * g4 + l15);
        const unsigned p01 = pkbf(a[2][0] * kscale, a[2][1] * kscale), p23 = pkbf(a[2][2] * kscale, a[2][3] * kscale);
        ST16(i3, p01); ST16(i3 + 32, p01 >> 16); ST16(i3 + 64, p23); ST16(i3 + 96, p23 >> 16);
        MEMF();
      }
      {
#pragma unroll
        for (int r = 2; r < 4; ++r) {
          const LAS volatile u16* a = (const LAS volatile u16*)(sm + tb + 2 * (512 + 64 * (r - 2) + lane));
          const unsigned lo = a[0], hi = a[1];
          const unsigned c = (lane & 1) ? hi : lo;
          ST128(kx + r * 1024 + lane * 16, (v4u{lo | (lo << 16), lo | (lo << 16), c | (hi << 16), hi | (hi << 16)}));
        }
        {
          const int a = tb + 2 * (384 + 2 * lane);
          const unsigned w0 = *(LAS const unsigned*)(sm + a), p = LD16(a + 4);
          ST128(kx + 1024 + lane * 16, (v4u{__builtin_amdgcn_perm(w0, w0, 0x01000100u), w0, __builtin_amdgcn_perm(w0, w0, 0x03020302u), p | (p << 16)}));
        }
        {
          const int a = tb + 2 * (256 + 4 * l31);
          const v2u wl = *(LAS const v2u*)(sm + a);
          const unsigned t4 = LD16(a + 8);
          v4u o = {__builtin_amdgcn_perm(wl.x, wl.x, 0x01000100u), __builtin_amdgcn_alignbit(wl.y, wl.x, 16),
                   __builtin_amdgcn_perm(wl.y, wl.y, 0x01000100u), (wl.y >> 16) | (t4 << 16)};
          if (hh) o = v4u{0u, 0u, 0u, 0u};
          if (lane < 34) ST128(hh ? kx - 32 + l31 * 16 : kx + 512 + lane * 16, o);
        }
      }
      sig_add(sm, S_KRDY, lane);
    } else if (w == 6) {
      __builtin_amdgcn_s_setprio(3);
      WAITV(2); SCHED();
      asm volatile("" : "+v"(pfw), "+v"(pfc) :: "memory");
      tag = __builtin_amdgcn_readfirstlane(pfw) + 0x9E3779B9u;

      sig_set(sm, S_TAG, tag);
      sig_set(sm, S_TAGOK, 1);
      wait_ge(sm, S_LNW, 4);
      {
        const int ta = L_U + (((lane >> 3) & 1) + 4 * ((lane >> 2) & 1)) * LNP + (g4 * 128 + ((lane >> 1) & 1) * 64 + (lane & 1) * 4) * 2;
        v4u t[8];
#pragma unroll
        for (int k = 0; k < 8; ++k) t[k] = trfrag(sm, ta + k * 16, 2 * LNP);
#pragma unroll
        for (int k = 0; k < 8; ++k) st16_sc1(t[k], (unsigned)(lane * 16), out + cp * 8192 + k * 1024);
        v2u sg = {*(LAS const unsigned*)(sm + L_STW + (lane & 7) * 4), tag};
        WAITV(0); SCHED();
        PIN(e0);
        if (lane < 8) st8_sc1(sg, (unsigned)(lane * 8), ws + WS_STAT + cp * 64);
      }
      unsigned pfd = 0;
#define TOUCH_WD() asm volatile("global_load_dword %0, %1, %2" : "+v"(pfd) : "v"(imin(lane, 42) * 128), "s"(wd + bid * 5504) : "memory")
      TOUCH_WD();
      asm volatile("global_load_dword %0, %1, %2 sc1" : "+v"(pfd) : "v"(imin(lane, 3) * 2097152 + 1048576), "s"(ws) : "memory");
#pragma unroll
      for (int i = 0; i < 2; ++i) if (l15 < 15) ST64(L_U + (4 * i + g4) * UP + l15 * 8, (v2u{0u, 0u}));
      if (lane < 2) ST128(L_MB + lane * 144, (v4u{0u, 0u, 0u, 0u}));
      wait_free(sm, idle1 ? 6 : GATHER_AT);
      v4u ug[8];
#pragma unroll 1
      for (unsigned spins = 0; spins < SPIN_CAP; ++spins) {
        unsigned f[4];
#pragma unroll
        for (int i = 0; i < 4; ++i) ld4_sc1(f[i], (unsigned)(lane * 64 + 4), ws + WS_STAT + i * 4096);
        asm volatile("s_waitcnt vmcnt(0)" : "+v"(f[0]), "+v"(f[1]), "+v"(f[2]), "+v"(f[3]) :: "memory");
        if (allv(((f[0] ^ tag) | (f[1] ^ tag) | (f[2] ^ tag) | (f[3] ^ tag)) == 0)) break;
        __builtin_amdgcn_s_sleep(8);
      }
#pragma unroll
      for (int i = 0; i < 8; ++i) ld16_sc1(ug[i], (unsigned)((lane >> 1) * 8192 + (lane & 1) * 16), out + ((cp >> 2) & 7) * 1024 + (cp >> 5) * 128 + (cp & 3) * 32 + i * (32 * 8192));
      asm volatile("s_waitcnt vmcnt(0)" : "+v"(ug[0]), "+v"(ug[1]), "+v"(ug[2]), "+v"(ug[3]), "+v"(ug[4]), "+v"(ug[5]), "+v"(ug[6]), "+v"(ug[7]) :: "memory");
      PIN(pfd);
      if (bid == 0) flag_st(tag, 0u, ws + WS_E);
      {
        const int ua = L_U + (lane & 1) * UCH + 120 + (lane >> 1) * 4;
#pragma unroll
        for (int i = 0; i < 8; ++i)
#pragma unroll
          for (int ph = 0; ph < 4; ++ph) *(LAS unsigned*)(sm + ua + ph * UP + i * 128) = ug[i][ph];
        sig_set(sm, S_URDY, 1);
      }
    }
    wait_ge(sm, S_TAGOK, 1);
    tag = lds_rd(sm, S_TAG);
    if (w == 5 || w == 6) {
      v4u sg; v2u sg2, cg[3];
#define LD_SC() do { ld16_sc1(sg, (unsigned)(lane * 16), ws + WS_STAT + rb * 1024); ld8_sc1(sg2, (unsigned)(lane * 8), ws + WS_STAT + tm * 512); \
        _Pragma("unroll") for (int i = 0; i < 3; ++i) ld8_sc1(cg[i], (unsigned)(lane * 8), ws + WS_CS + cb * 2048 + i * 512); } while (0)
#define OK_SC() allv(((sg.y ^ tag) | (sg.w ^ tag) | (sg2.y ^ tag) | (cg[0].y ^ tag) | (cg[1].y ^ tag) | (cg[2].y ^ tag)) == 0)
#define PUT_SC() do { \
        ST16(L_SD + lane * 2, sg2.x >> 16); \
        if (!idle1) { \
          const unsigned n0 = sg.x & 0xffffu, n1 = sg.z & 0xffffu; \
          ST128(L_NM + lane * 16, (v4u{n0 | (n0 << 16), n0, n1 | (n1 << 16), n1})); \
          _Pragma("unroll") for (int i = 0; i < 3; ++i) { \
            float c = __builtin_bit_cast(float, cg[i].x); \
            float c0 = rbf(c), c1 = rbf(c - c0), c2 = (c - c0) - c1; \
            ST64(L_CSX + (64 * i + lane) * 8, (v2u{pkbf(c0, c1), pkbf(c2, 0.f)})); \
          } \
        } \
        sig_set(sm, S_CS, 1); } while (0)
      __builtin_amdgcn_s_setprio(3);
      wait_free(sm, SC_AT);
#pragma unroll 1
      for (unsigned spins = 0; lds_rd(sm, S_CS) == 0; ++spins) {
        LD_SC();
        asm volatile("s_waitcnt vmcnt(0)" : "+v"(sg), "+v"(sg2), "+v"(cg[0]), "+v"(cg[1]), "+v"(cg[2]) :: "memory");
        if (OK_SC() || spins >= SPIN_CAP) { PUT_SC(); break; }
      }
    }
    wait_ge(sm, S_URDY, 1);
    {
      wait_ge(sm, S_KRDY, 2);
      __builtin_amdgcn_s_setprio(CPRIO);
      const int bp = (((lane >> 1) & 15) + 16 * (2 * (lane & 1) + hh)) * 4;
      const int bq = (g4 ? lane - 16 : lane + 47) * 4;
      const int ch = w & 1;
      const int t01 = w < 6 ? 0 : 2, t23 = w < 6 ? 6 : 4;
      f32x4 y[4], cy[4];
      {
        const unsigned ka = lb + L_KX + ch * KXCH + 224 - 64 * g4 - 8 * ((lane >> 2) & 3) + 8 * (lane & 3);
        const unsigned ub = lb + L_U + ch * UCH + 8 * l15 + 16 * g4;
        f32x4 d[16];
#define CV_OPS "=&v"(d[0]), "=&v"(d[1]), "=&v"(d[2]), "=&v"(d[3]), "=&v"(d[4]), "=&v"(d[5]), "=&v"(d[6]), "=&v"(d[7]), \
               "=&v"(d[8]), "=&v"(d[9]), "=&v"(d[10]), "=&v"(d[11]), "=&v"(d[12]), "=&v"(d[13]), "=&v"(d[14]), "=&v"(d[15]) : "v"(ka), "v"(ub)
        if (w < 6) asm volatile(CVA_ASM : CV_OPS : "memory", CVA_CLOB);
        else asm volatile(CVB_ASM : CV_OPS, "v"(lb + S_FULL), "v"(w == 7 ? 52 : 0) : "memory", CVB_CLOB);
        __builtin_amdgcn_s_setprio(3);
#pragma unroll
        for (int i = 0; i < 4; ++i) {
          const f32x4 d0 = d[i], d1 = d[4 + i], d2 = d[8 + i], d3 = d[12 + i];
          cy[i] = f32x4{(d1.w + d2.z) + d3.y, d2.w + d3.z, d3.w, 0.f};
          y[i] = f32x4{d0.x, d0.y + d1.x, (d0.z + d1.y) + d2.x, (d0.w + d1.z) + (d2.y + d3.x)};
        }
        if (lane == 63) {
#pragma unroll
          for (int i = 0; i < 4; ++i) *(LAS f32x4*)(sm + L_MB + ch * 144 + ((i < 2 ? t01 : t23 - 2) + i + 1) * 16) = cy[i];
        }
      }
      sig_add(sm, S_YCNT + ch * 4, lane);
#pragma unroll
      for (int i = 0; i < 4; ++i)
#pragma unroll
        for (int k = 0; k < 3; ++k) {
          const float e = cy[i][k];
          float v = __builtin_bit_cast(float, __builtin_amdgcn_ds_bpermute(bq, __builtin_bit_cast(int, e)));
          PIN(v);
          cy[i][k] = v;
        }
      wait_ge(sm, S_YCNT + ch * 4, 2);
      v2u o[4];
#pragma unroll
      for (int i = 0; i < 4; ++i) {
        const f32x4 mb = *(LAS const f32x4*)(sm + L_MB + ch * 144 + ((i < 2 ? t01 : t23 - 2) + i) * 16);
        const f32x4 c = lane == 0 ? mb : cy[i];
        o[i] = v2u{pkbf(y[i].x + c.x, y[i].y + c.y), pkbf(y[i].z + c.z, y[i].w)};
      }
#pragma unroll
      for (int i = 0; i < 4; i += 2) {
        auto r0 = __builtin_amdgcn_permlane16_swap(o[i].x, o[i + 1].x, false, false);
        auto r1 = __builtin_amdgcn_permlane16_swap(o[i].y, o[i + 1].y, false, false);
        v4u q = {(unsigned)__builtin_amdgcn_ds_bpermute(bp, r0[0]), (unsigned)__builtin_amdgcn_ds_bpermute(bp, r1[0]),
                 (unsigned)__builtin_amdgcn_ds_bpermute(bp, r0[1]), (unsigned)__builtin_amdgcn_ds_bpermute(bp, r1[1])};
        st16_sc1(q, (unsigned)(lane * 16), ws + WS_YG + (size_t)(2 * cp + ch) * 4096 + (i ? t23 : t01) * 512);
      }
      WAITV(0);
      if (add_ret(sm, S_CONV) == 3) flag_st(tag, (unsigned)((cp >> 3) * 2048 + (cp & 7) * F4S), ws + WS_F4);
    }
    {
      const int xw = w - 4;
      v4u xr[2], yg[2];
      const char* xb2 = x + tm * 65536 + tn * 128;
      const unsigned xo = (unsigned)((8 * xw + (lane >> 3)) * 1024 + (lane & 7) * 16);
      ld16(xr[0], xo, xb2); ld16(xr[1], xo, xb2 + 32768);
      wait_free(sm, 8);
      unsigned f;
#pragma unroll 1
      for (unsigned spins = 0; spins < SPIN_CAP; ++spins) {
        ld4_sc1(f, (unsigned)((lane & 7) * F4S), ws + WS_F4 + tn * 8192 + xw * 2048);
        asm volatile("s_waitcnt vmcnt(0)" : "+v"(f) :: "memory");
        if (allv(f == tag)) break;
        __builtin_amdgcn_s_sleep(F4SLEEP);
      }
      const unsigned ygo = (unsigned)((16 * xw + (lane >> 3)) * 4096 + (lane & 7) * 16);
      const char* ygbase = ws + WS_YG + (size_t)tn * 64 * 4096 + tm * 128;
      ld16_sc1(yg[0], ygo, ygbase); ld16_sc1(yg[1], ygo, ygbase + 8 * 4096);
      SCHED();
      asm volatile("" : "+v"(xr[0]), "+v"(xr[1]));
      ST128(L_XIMG + xw * 1024 + lane * 16, xr[0]); ST128(L_XIMG + 4096 + xw * 1024 + lane * 16, xr[1]);
      asm volatile("s_waitcnt vmcnt(0)" : "+v"(yg[0]), "+v"(yg[1]) :: "memory");
#pragma unroll
      for (int i = 0; i < 2; ++i) {
        const int ab = L_AIMG + (lane & 7) * 1024 + (((2 * xw + i) ^ (lane & 7)) << 4) + (lane >> 3) * 2;
#pragma unroll
        for (int j = 0; j < 4; ++j) { ST16(ab + j * 256, yg[i][j]); ST16(ab + j * 256 + 128, yg[i][j] >> 16); }
      }
    }
  }
  BARRIER();
  {
    const int tok = tid >> 3, c = tid & 7;
    const LAS f32x4* rp = (const LAS f32x4*)(sm + L_RED + tok * REDP + c * 32);
    f32x4 s0 = rp[0] + rp[REDQ / 16], s1 = rp[1] + rp[REDQ / 16 + 1];
    const float rs = __builtin_amdgcn_rcpf(bflo(LD16(L_SD + tok * 2))), rs2 = rs * rs;
    const v4u av = LD128(L_AIMG + tok * 128 + ((c ^ (tok >> 3)) << 4)), xx = LD128(L_XIMG + tid * 16);
    const float g[8] = {s0.x, s0.y, s0.z, s0.w, s1.x, s1.y, s1.z, s1.w};
    v4u o;
#pragma unroll
    for (int j = 0; j < 4; ++j)
      o[j] = pkbf(__builtin_fmaf(g[2 * j], rs2, bflo(xx[j]) + bflo(av[j])), __builtin_fmaf(g[2 * j + 1], rs2, bfhi(xx[j]) + bfhi(av[j])));
    st16(o, (unsigned)(tok * 1024 + c * 16), out + tm * 65536 + tn * 128);
  }
}

extern "C" void kernel_launch(void* const* d_in, const int* in_sizes, int n_in,
                              void* d_out, int out_size, void* d_ws, size_t ws_size,
                              hipStream_t stream) {
  fused_kernel<<<256, 512, 0, stream>>>(
      (const u16*)d_in[0], (const u16*)d_in[1], (const u16*)d_in[2], (const u16*)d_in[3], (const u16*)d_in[4],
      (const u16*)d_in[5], (const u16*)d_in[6], (const u16*)d_in[8], (const u16*)d_in[9], (const u16*)d_in[10],
      (u16*)d_out, (char*)d_ws);
}
```
